```python
import torch

def _softmax(x, axis=-1):
    return torch.softmax(x, dim=axis)
def _gelu(x):
    return torch.nn.functional.gelu(x, approximate="tanh")
def _silu(x):
    return torch.nn.functional.silu(x)
def _sigmoid(x):
    return torch.sigmoid(x)
def _layernorm(x, axis=-1, eps=1e-5):
    m = x.mean(dim=axis, keepdim=True)
    v = x.var(dim=axis, keepdim=True, unbiased=False)
    return (x - m) / torch.sqrt(v + eps)
def _scatter_add(tgt, idx, src, axis=0):
    return tgt.clone().index_add_(axis, idx, src)
def _segment_sum(src, seg, n):
    out = torch.zeros((n,) + src.shape[1:], dtype=src.dtype)
    return out.index_add_(0, seg, src)
def _pad(x, pw):
    flat = tuple(v for pair in reversed(pw) for v in pair)
    return torch.nn.functional.pad(x, flat)

kernel_name = 'standalone__window_attn_swin__vision'

def setup_inputs():

    _w = (lambda a: (lambda t: t.to(torch.bfloat16) if t.is_floating_point() else t)(torch.as_tensor(a)))
    H = W = 224
    C = 192
    ws = 8
    nh = 4
    shift = ws // 2
    coords = torch.stack(torch.meshgrid(torch.arange(ws), torch.arange(ws), indexing='ij'), 0).reshape(2, -1)
    rel = coords[:, :, None] - coords[:, None, :]
    rel[0] += ws - 1
    rel[1] += ws - 1
    rel[0] *= 2 * ws - 1
    rp_idx = rel.sum(0).to(torch.int64)
    img_mask = torch.zeros((1, H, W, 1), dtype=float)
    cnt = 0
    for hs in (slice(0, H - ws), slice(H - ws, H - shift), slice(H - shift, H)):
        for vs in (slice(0, W - ws), slice(W - ws, W - shift), slice(W - shift, W)):
            img_mask[:, hs, vs, :] = cnt
            cnt += 1
    mw = img_mask.reshape(1, H // ws, ws, W // ws, ws, 1).permute(0, 1, 3, 2, 4, 5).reshape(-1, ws * ws)
    am = mw[:, :, None] - mw[:, None, :]
    attn_mask = torch.where(am != 0, -100.0, 0.0).to(float)
    return {
        "x": _w((lambda *s: torch.randn(*s, dtype=torch.bfloat16) * 1.6)(1, H, W, C)),
        "wqkv": _w((lambda *s: torch.randn(*s, dtype=torch.bfloat16) * 1.6)(C, 3 * C) * 0.02),
        "wo": _w((lambda *s: torch.randn(*s, dtype=torch.bfloat16) * 0.05)(C, C) * 0.02),
        "rpb": _w((lambda *s: torch.randn(*s, dtype=torch.bfloat16) * 0.2)((2 * ws - 1) * (2 * ws - 1), nh)),
        "rp_idx": _w(rp_idx),
        "attn_mask": _w(attn_mask),
    }

def reference(x, wqkv, wo, rpb, rp_idx, attn_mask):
    B, H, W, C = x.shape
    N = rp_idx.shape[0]
    ws = int(round(N ** 0.5))
    nh = rpb.shape[-1]
    hd = C // nh
    shift = ws // 2
    xs = torch.roll(x, (-shift, -shift), (1, 2))
    nWh = H // ws
    nWw = W // ws
    xw = (lambda x, *d: x.permute(*d))(xs.reshape(B, nWh, ws, nWw, ws, C), 0, 1, 3, 2, 4, 5).reshape(B * nWh * nWw, N, C)
    qkv = (lambda x, *d: x.permute(*d))(torch.einsum('wnc,cd->wnd', xw, wqkv).reshape(-1, N, 3, nh, hd), 2, 0, 3, 1, 4)
    q, k, v = qkv[0], qkv[1], qkv[2]
    attn = torch.einsum('whid,whjd->whij', q, k) * (hd ** -0.5)
    bias = (lambda x, *d: x.permute(*d))(rpb[rp_idx.reshape(-1)].reshape(N, N, nh), 2, 0, 1)
    attn = attn + bias[None, :, :, :]
    attn = (attn.reshape(B, nWh * nWw, nh, N, N) + attn_mask[None, :, None, :, :]).reshape(-1, nh, N, N)
    attn = _softmax(attn, axis=-1)
    out = (lambda x, *d: x.permute(*d))(torch.einsum('whij,whjd->whid', attn, v), 0, 2, 1, 3).reshape(-1, N, C)
    out = torch.einsum('wnc,cd->wnd', out, wo)
    out = (lambda x, *d: x.permute(*d))(out.reshape(B, nWh, nWw, ws, ws, C), 0, 1, 3, 2, 4, 5).reshape(B, H, W, C)
    return torch.roll(out, (shift, shift), (1, 2))
```

```cpp
#include <hip/hip_runtime.h>
#include <hip/hip_bf16.h>

typedef unsigned short u16;
__device__ __forceinline__ float bf2f(u16 v) { return __uint_as_float(((unsigned)v) << 16); }
__device__ __forceinline__ u16 f2bf(float f) { __bf16 b = (__bf16)f; return __builtin_bit_cast(u16, b); }

__global__ __launch_bounds__(256) void naive_kernel(const u16* __restrict__ x, const u16* __restrict__ wqkv, const u16* __restrict__ wo,
                                                    const u16* __restrict__ rpb, const int* __restrict__ rpidx, const u16* __restrict__ mask, u16* __restrict__ out) {
  __shared__ u16 sx[64 * 192];
  __shared__ u16 sqkv[64 * 576];
  __shared__ float sp[64 * 65];
  const int w = blockIdx.x, wy = w / 28, wx = w % 28, tid = threadIdx.x;
  for (int i = tid; i < 64 * 192; i += 256) {
    int t = i / 192, c = i % 192;
    int yy = (wy * 8 + (t >> 3) + 4) % 224, xx = (wx * 8 + (t & 7) + 4) % 224;
    sx[i] = x[(yy * 224 + xx) * 192 + c];
  }
  __syncthreads();
  for (int i = tid; i < 64 * 576; i += 256) {
    int t = i / 576, n = i % 576;
    float acc = 0.f;
    for (int c = 0; c < 192; ++c) acc += bf2f(sx[t * 192 + c]) * bf2f(wqkv[c * 576 + n]);
    sqkv[i] = f2bf(acc);
  }
  __syncthreads();
  const float scale = 0.14433756729740643f;
  for (int h = 0; h < 4; ++h) {
    for (int i = tid; i < 4096; i += 256) {
      int qi = i >> 6, kj = i & 63;
      float acc = 0.f;
      for (int d = 0; d < 48; ++d) acc += bf2f(sqkv[qi * 576 + h * 48 + d]) * bf2f(sqkv[kj * 576 + 192 + h * 48 + d]);
      acc = acc * scale + bf2f(rpb[rpidx[i] * 4 + h]) + bf2f(mask[w * 4096 + i]);
      sp[qi * 65 + kj] = acc;
    }
    __syncthreads();
    if (tid < 64) {
      float m = -INFINITY;
      for (int j = 0; j < 64; ++j) m = fmaxf(m, sp[tid * 65 + j]);
      float s = 0.f;
      for (int j = 0; j < 64; ++j) { float e = __expf(sp[tid * 65 + j] - m); sp[tid * 65 + j] = e; s += e; }
      float r = 1.f / s;
      for (int j = 0; j < 64; ++j) sp[tid * 65 + j] = bf2f(f2bf(sp[tid * 65 + j] * r));
    }
    __syncthreads();
    for (int i = tid; i < 64 * 48; i += 256) {
      int qi = i / 48, d = i % 48;
      float acc = 0.f;
      for (int j = 0; j < 64; ++j) acc += sp[qi * 65 + j] * bf2f(sqkv[j * 576 + 384 + h * 48 + d]);
      sx[qi * 192 + h * 48 + d] = f2bf(acc);
    }
    __syncthreads();
  }
  for (int i = tid; i < 64 * 192; i += 256) {
    int t = i / 192, n = i % 192;
    float acc = 0.f;
    for (int c = 0; c < 192; ++c) acc += bf2f(sx[t * 192 + c]) * bf2f(wo[c * 192 + n]);
    int yy = (wy * 8 + (t >> 3) + 4) % 224, xx = (wx * 8 + (t & 7) + 4) % 224;
    out[(yy * 224 + xx) * 192 + n] = f2bf(acc);
  }
}

extern "C" void kernel_launch(void* const* d_in, const int* in_sizes, int n_in, void* d_out, int out_size, void* d_ws, size_t ws_size, hipStream_t stream) {
  naive_kernel<<<784, 256, 0, stream>>>((const u16*)d_in[0], (const u16*)d_in[1], (const u16*)d_in[2], (const u16*)d_in[3], (const int*)d_in[4], (const u16*)d_in[5], (u16*)d_out);
}
```

```cpp
#include <hip/hip_runtime.h>
#include <hip/hip_bf16.h>

typedef unsigned short u16;
__device__ __forceinline__ float bf2f(u16 v) { return __uint_as_float(((unsigned)v) << 16); }
__device__ __forceinline__ u16 f2bf(float f) { __bf16 b = (__bf16)f; return __builtin_bit_cast(u16, b); }

__global__ __launch_bounds__(256) void naive_kernel(const u16* __restrict__ x, const u16* __restrict__ wqkv, const u16* __restrict__ wo,
                                                    const u16* __restrict__ rpb, const int* __restrict__ rpidx, const u16* __restrict__ mask, u16* __restrict__ out) {
  __shared__ u16 sx[64 * 192];
  __shared__ u16 sqkv[64 * 576];
  __shared__ float sp[64 * 65];
  const int tid = threadIdx.x; unsigned long long r0 = __builtin_amdgcn_s_memrealtime();
  for (int w = blockIdx.x; w < 784; w += 256) { const int wy = w / 28, wx = w % 28;
  for (int i = tid; i < 64 * 192; i += 256) {
    int t = i / 192, c = i % 192;
    int yy = (wy * 8 + (t >> 3) + 4) % 224, xx = (wx * 8 + (t & 7) + 4) % 224;
    sx[i] = x[(yy * 224 + xx) * 192 + c];
  }
  __syncthreads();
  for (int i = tid; i < 64 * 576; i += 256) {
    int t = i / 576, n = i % 576;
    float acc = 0.f;
    for (int c = 0; c < 192; ++c) acc += bf2f(sx[t * 192 + c]) * bf2f(wqkv[c * 576 + n]);
    sqkv[i] = f2bf(acc);
  }
  __syncthreads();
  const float scale = 0.14433756729740643f;
  for (int h = 0; h < 4; ++h) {
    for (int i = tid; i < 4096; i += 256) {
      int qi = i >> 6, kj = i & 63;
      float acc = 0.f;
      for (int d = 0; d < 48; ++d) acc += bf2f(sqkv[qi * 576 + h * 48 + d]) * bf2f(sqkv[kj * 576 + 192 + h * 48 + d]);
      acc = acc * scale + bf2f(rpb[rpidx[i] * 4 + h]) + bf2f(mask[w * 4096 + i]);
      sp[qi * 65 + kj] = acc;
    }
    __syncthreads();
    if (tid < 64) {
      float m = -INFINITY;
      for (int j = 0; j < 64; ++j) m = fmaxf(m, sp[tid * 65 + j]);
      float s = 0.f;
      for (int j = 0; j < 64; ++j) { float e = __expf(sp[tid * 65 + j] - m); sp[tid * 65 + j] = e; s += e; }
      float r = 1.f / s;
      for (int j = 0; j < 64; ++j) sp[tid * 65 + j] = bf2f(f2bf(sp[tid * 65 + j] * r));
    }
    __syncthreads();
    for (int i = tid; i < 64 * 48; i += 256) {
      int qi = i / 48, d = i % 48;
      float acc = 0.f;
      for (int j = 0; j < 64; ++j) acc += sp[qi * 65 + j] * bf2f(sqkv[j * 576 + 384 + h * 48 + d]);
      sx[qi * 192 + h * 48 + d] = f2bf(acc);
    }
    __syncthreads();
  }
  for (int i = tid; i < 64 * 192; i += 256) {
    int t = i / 192, n = i % 192;
    float acc = 0.f;
    for (int c = 0; c < 192; ++c) acc += bf2f(sx[t * 192 + c]) * bf2f(wo[c * 192 + n]);
    int yy = (wy * 8 + (t >> 3) + 4) % 224, xx = (wx * 8 + (t & 7) + 4) % 224;
    __builtin_nontemporal_store(f2bf(acc), &out[(yy * 224 + xx) * 192 + n]);
  }
  __syncthreads();
  }
  while (__builtin_amdgcn_s_memrealtime() - r0 < 600000ull) __builtin_amdgcn_s_sleep(1);
}

extern "C" void kernel_launch(void* const* d_in, const int* in_sizes, int n_in, void* d_out, int out_size, void* d_ws, size_t ws_size, hipStream_t stream) {
  naive_kernel<<<256, 256, 0, stream>>>((const u16*)d_in[0], (const u16*)d_in[1], (const u16*)d_in[2], (const u16*)d_in[3], (const int*)d_in[4], (const u16*)d_in[5], (u16*)d_out);
}
```

```cpp
#include <hip/hip_runtime.h>

__global__ __launch_bounds__(256, 1) void swin_attn_kernel(const void* x, const void* wqkv, const void* wo, const void* rpb,
                                                           const void* idx, const void* mask, void* out) {
  __shared__ __attribute__((aligned(16))) char smem[163840];
  unsigned tid = threadIdx.x;
  asm volatile(
    "s_load_dwordx8 s[0:7], %1, 0x0\n"
    "s_load_dwordx4 s[8:11], %1, 0x20\n"
    "s_load_dwordx2 s[12:13], %1, 0x30\n"
    "s_mov_b32 s14, %2\n"
    "v_mov_b32 v255, %0\n"
    "v_and_b32 v224, 63, v255\n"
    "v_lshrrev_b32 v242, 6, v255\n"
    "v_lshrrev_b32 v226, 4, v224\n"
    "v_readfirstlane_b32 s77, v242\n"
    "v_bfe_u32 v243, v224, 3, 2\n"
    "v_lshrrev_b32 v244, 5, v224\n"
    "v_and_b32 v245, 7, v224\n"
    "v_xor_b32 v246, v245, v226\n"
    "v_lshlrev_b32 v8, 4, v246\n"
    "v_mul_u32_u24 v246, 86016, v244\n"
    "v_mul_u32_u24 v6, 384, v243\n"
    "v_add3_u32 v6, v6, v246, v8\n"
    "v_xor_b32 v7, 64, v6\n"
    "v_add_u32 v7, 172032, v7\n"
    "s_mul_i32 s33, s77, 6144\n"
    "s_cmp_lt_u32 s14, 64\n"
    "s_cbranch_scc1 .Lswin_nowarm%=\n"
    "s_getpc_b64 s[36:37]\n"
    "s_lshr_b32 s80, s14, 3\n"
    "s_sub_u32 s80, s80, 8\n"
    "v_lshlrev_b32 v236, 4, v255\n"
    "s_lshl_b32 s83, s80, 12\n"
    "s_and_b32 s36, s36, 0xfffffc00\n"
    "s_and_b32 s37, s37, 0xffff\n"
    "s_mov_b32 s38, 24656\n"
    "s_mov_b32 s39, 0x20000\n"
    "v_add_u32 v237, s83, v236\n"
    "buffer_load_dwordx4 a[240:243], v237, s[36:39], 0 offen\n"
    "s_sub_u32 s81, s80, 18\n"
    "s_mov_b32 s82, 221184\n"
    "s_cmp_lt_u32 s80, 18\n"
    "s_cselect_b32 s42, s82, 73728\n"
    "s_cselect_b32 s81, s80, s81\n"
    "s_mov_b32 s43, 0x20000\n"
    "s_mul_i32 s81, s81, 12288\n"
    "v_add_u32 v238, s81, v236\n"
    "v_add_u32 v237, 4294934528, v237\n"
    "s_movk_i32 s81, 4096\n"
    "s_movk_i32 s82, 8192\n"
    "s_mov_b32 s38, 16384\n"
    "s_and_b32 s88, s14, 63\n"
    "s_mul_hi_u32 s89, s88, 0x924924a\n"
    "s_mul_i32 s90, s89, 28\n"
    "s_sub_u32 s90, s88, s90\n"
    "s_lshr_b32 s91, s77, 1\n"
    "s_lshl_b32 s89, s89, 1\n"
    "s_add_u32 s89, s89, s91\n"
    "s_add_u32 s89, s89, 1\n"
    "s_mul_i32 s91, s89, 344064\n"
    "s_and_b32 s93, s77, 1\n"
    "s_lshl_b32 s90, s90, 1\n"
    "s_add_u32 s90, s90, s93\n"
    "s_add_u32 s90, s90, 1\n"
    "s_cmp_eq_u32 s90, 56\n"
    "s_cselect_b32 s90, 0, s90\n"
    "s_mul_i32 s93, s90, 1536\n"
    "s_add_u32 s91, s91, s93\n"
    "s_lshr_b32 s92, s14, 6\n"
    "s_sub_u32 s92, s92, 1\n"
    "s_lshl_b32 s94, s92, 7\n"
    "s_add_u32 s91, s91, s94\n"
    "s_lshl_b32 s88, s88, 13\n"
    "s_lshl_b32 s92, s92, 12\n"
    "s_add_u32 s88, s88, s92\n"
    "s_cmp_lt_u32 s14, 192\n"
    "s_cselect_b32 s94, 6422528, 0\n"
    "s_waitcnt lgkmcnt(0)\n"
    "s_mov_b32 s16, s0\n"
    "s_and_b32 s17, s1, 0xffff\n"
    "s_mov_b32 s18, 19267584\n"
    "s_mov_b32 s19, 0x20000\n"
    "buffer_load_dwordx4 a[224:227], v6, s[16:19], s91 offen nt\n"
    "buffer_load_dwordx4 a[228:231], v7, s[16:19], s91 offen nt\n"
    "s_mov_b32 s16, s10\n"
    "s_and_b32 s17, s11, 0xffff\n"
    "s_mov_b32 s18, s94\n"
    "buffer_load_dwordx4 a[220:223], v236, s[16:19], s88 offen nt\n"
    "s_cmp_lt_u32 s80, 18\n"
    "s_cselect_b32 s40, s2, s4\n"
    "s_cselect_b32 s41, s3, s5\n"
    "s_and_b32 s41, s41, 0xffff\n"
    "buffer_load_dwordx4 a[244:247], v238, s[40:43], 0 offen\n"
    "buffer_load_dwordx4 a[248:251], v238, s[40:43], s81 offen\n"
    "buffer_load_dwordx4 a[252:255], v238, s[40:43], s82 offen\n"
    "s_mov_b32 s36, s8\n"
    "s_and_b32 s37, s9, 0xffff\n"
    "buffer_load_dwordx4 a[236:239], v237, s[36:39], 0 offen\n"
    "s_sleep 24\n"
    ".Lswin_nowarm%=:\n"
    "s_mul_hi_u32 s89, s14, 0x924924a\n"
    "s_mul_i32 s90, s89, 28\n"
    "s_sub_u32 s90, s14, s90\n"
    "s_mul_i32 s91, s89, 688128\n"
    "s_add_u32 s92, s91, 688128\n"
    "s_add_u32 s91, s91, 344064\n"
    "s_cmp_eq_u32 s89, 27\n"
    "s_cselect_b32 s92, 0, s92\n"
    "s_mul_i32 s93, s90, 3072\n"
    "s_add_u32 s94, s93, 3072\n"
    "s_add_u32 s93, s93, 1536\n"
    "s_cmp_eq_u32 s90, 27\n"
    "s_cselect_b32 s94, 0, s94\n"
    "s_cmp_ge_u32 s77, 2\n"
    "s_cselect_b32 s88, s92, s91\n"
    "s_bitcmp1_b32 s77, 0\n"
    "s_cselect_b32 s89, s94, s93\n"
    "s_add_u32 s64, s88, s89\n"
    "v_lshrrev_b32 v225, 3, v255\n"
    "v_mul_u32_u24 v232, 1152, v225\n"
    "v_and_b32 v246, 2, v225\n"
    "v_bfe_u32 v242, v225, 3, 1\n"
    "v_lshl_or_b32 v246, v242, 2, v246\n"
    "v_xor_b32 v246, v246, v245\n"
    "v_lshl_add_u32 v232, v246, 4, v232\n"
    "s_lshr_b32 s34, s33, 1\n"
    "v_lshlrev_b32 v229, 4, v224\n"
    "s_lshl_b32 s15, s77, 10\n"
    "s_waitcnt lgkmcnt(0)\n"
    "s_mov_b32 s36, s8\n"
    "s_and_b32 s37, s9, 0xffff\n"
    "s_mov_b32 s38, 16384\n"
    "s_mov_b32 s39, 0x20000\n"
    "s_lshl_b32 s35, s15, 2\n"
    "buffer_load_dwordx4 v[40:43], v229, s[36:39], s35 offen offset:0\n"
    "s_add_u32 m0, s15, 94208\n"
    "s_mov_b32 s84, s6\n"
    "s_and_b32 s85, s7, 0xffff\n"
    "s_mov_b32 s86, 1800\n"
    "s_mov_b32 s87, 0x20000\n"
    "buffer_load_dwordx4 v[44:47], v229, s[36:39], s35 offen offset:1024\n"
    "s_mov_b32 s16, s0\n"
    "s_and_b32 s17, s1, 0xffff\n"
    "s_mov_b32 s18, 19267584\n"
    "s_mov_b32 s19, 0x20000\n"
    "s_lshr_b32 s79, s77, 1\n"
    "buffer_load_dwordx4 v[48:51], v229, s[36:39], s35 offen offset:2048\n"
    "s_and_b32 s72, s77, 1\n"
    "v_lshl_add_u32 v243, v244, 3, v243\n"
    "s_lshl_b32 s79, s79, 12\n"
    "s_lshl_b32 s72, s72, 9\n"
    "v_lshl_add_u32 v8, v243, 7, v8\n"
    "buffer_load_dwordx4 v[52:55], v229, s[36:39], s35 offen offset:3072\n"
    "s_add_u32 s79, s79, s72\n"
    "v_add_u32 v8, s79, v8\n"
    "s_lshl_b32 s79, s77, 11\n"
    "s_add_u32 s79, s79, 24576\n"
    "s_mov_b32 s24, s10\n"
    "buffer_load_dwordx4 v229, s[84:87], s15 offen lds\n"
    "s_add_u32 m0, s33, 0\n"
    "s_and_b32 s25, s11, 0xffff\n"
    "s_mov_b32 s26, 6422528\n"
    "s_mov_b32 s27, 0x20000\n"
    "buffer_load_dwordx4 v6, s[16:19], s64 offen nt lds\n"
    "s_add_u32 m0, s33, 1024\n"
    "s_lshl_b32 s72, s14, 13\n"
    "v_xor_b32 v9, 64, v8\n"
    "v_add_u32 v9, 2048, v9\n"
    "buffer_load_dwordx4 v7, s[16:19], s64 offen nt lds\n"
    "s_add_u32 m0, s33, 1920\n"
    "s_mov_b32 s28, s2\n"
    "s_and_b32 s29, s3, 0xffff\n"
    "s_mov_b32 s30, 221184\n"
    "buffer_load_dwordx4 v6, s[16:19], s64 offen offset:128 nt lds\n"
    "s_add_u32 m0, s33, 2944\n"
    "s_mov_b32 s31, 0x20000\n"
    "s_mov_b32 s35, 0\n"
    "v_and_b32 v242, 6, v225\n"
    "buffer_load_dwordx4 v7, s[16:19], s64 offen offset:128 nt lds\n"
    "s_add_u32 m0, s33, 3840\n"
    "v_xor_b32 v242, v242, v245\n"
    "v_mul_u32_u24 v241, 384, v225\n"
    "v_lshl_add_u32 v241, v242, 4, v241\n"
    "buffer_load_dwordx4 v6, s[16:19], s64 offen offset:256 nt lds\n"
    "s_add_u32 m0, s33, 4864\n"
    "s_add_u32 s44, s91, s93\n"
    "s_add_u32 s45, s91, s94\n"
    "v_bfe_u32 v242, v224, 2, 2\n"
    "buffer_load_dwordx4 v7, s[16:19], s64 offen offset:256 nt lds\n"
    "s_add_u32 m0, s79, 0\n"
    "s_mul_i32 s88, s77, 3\n"
    "s_add_u32 s46, s92, s93\n"
    "s_add_u32 s47, s92, s94\n"
    "buffer_load_dwordx4 v8, s[24:27], s72 offen nt lds\n"
    "s_add_u32 m0, s79, 1024\n"
    "v_and_b32 v246, 1, v226\n"
    "v_lshrrev_b32 v244, 1, v242\n"
    "s_add_u32 s91, s88, 0\n"
    "buffer_load_dwordx4 v9, s[24:27], s72 offen nt lds\n"
    "s_add_u32 m0, s34, 32768\n"
    "v_and_b32 v225, 15, v224\n"
    "v_mul_u32_u24 v245, 3072, v226\n"
    "v_lshl_or_b32 v246, v246, 1, v244\n"
    "buffer_load_dwordx4 v232, s[28:31], s35 offen lds\n"
    "s_add_u32 m0, s34, 33664\n"
    "s_and_b32 s92, s91, 3\n"
    "v_and_b32 v243, 3, v224\n"
    "v_lshl_add_u32 v245, v242, 7, v245\n"
    "buffer_load_dwordx4 v232, s[28:31], s35 offen offset:128 lds\n"
    "s_add_u32 m0, s34, 34560\n"
    "s_lshr_b32 s91, s91, 2\n"
    "v_lshrrev_b32 v244, 1, v225\n"
    "v_lshl_add_u32 v245, v243, 3, v245\n"
    "buffer_load_dwordx4 v232, s[28:31], s35 offen offset:256 lds\n"
    "s_add_u32 m0, s34, 45056\n"
    "s_mov_b32 s35, 384\n"
    "s_lshl_b32 s91, s91, 10\n"
    "v_xor_b32 v216, s92, v246\n"
    "v_xor_b32 v244, v244, v226\n"
    "s_add_u32 s91, s91, 32768\n"
    "v_lshl_add_u32 v216, v216, 5, v245\n"
    "v_lshlrev_b32 v244, 4, v244\n"
    "v_add_u32 v216, s91, v216\n"
    "v_lshl_add_u32 v0, v225, 7, v244\n"
    "s_add_u32 s91, s88, 1\n"
    "s_and_b32 s92, s91, 3\n"
    "s_lshr_b32 s91, s91, 2\n"
    "s_lshl_b32 s91, s91, 10\n"
    "v_xor_b32 v217, s92, v246\n"
    "s_add_u32 s91, s91, 32768\n"
    "v_lshl_add_u32 v217, v217, 5, v245\n"
    "v_add_u32 v217, s91, v217\n"
    "s_add_u32 s91, s88, 2\n"
    "s_and_b32 s92, s91, 3\n"
    "s_lshr_b32 s91, s91, 2\n"
    "s_lshl_b32 s91, s91, 10\n"
    "v_xor_b32 v218, s92, v246\n"
    "s_add_u32 s91, s91, 32768\n"
    "v_lshl_add_u32 v218, v218, 5, v245\n"
    "v_add_u32 v218, s91, v218\n"
    "s_mov_b32 s83, 94208\n"
    "v_add_u32 v248, 69632, v216\n"
    "v_xor_b32 v1, 64, v0\n"
    "v_add_u32 v249, 69632, v217\n"
    "v_add_u32 v250, 69632, v218\n"
    "v_lshrrev_b32 v245, 1, v226\n"
    "v_and_b32 v244, 1, v226\n"
    "v_mul_u32_u24 v245, 3072, v245\n"
    "v_lshl_add_u32 v244, v244, 2, v242\n"
    "v_lshl_add_u32 v245, v244, 7, v245\n"
    "v_lshrrev_b32 v244, 1, v243\n"
    "v_add_u32 v244, s88, v244\n"
    "v_lshrrev_b32 v221, 2, v244\n"
    "v_and_b32 v244, 3, v244\n"
    "v_lshl_add_u32 v221, v221, 10, v245\n"
    "v_xor_b32 v244, v244, v246\n"
    "v_lshl_add_u32 v221, v244, 5, v221\n"
    "v_and_b32 v244, 1, v243\n"
    "s_add_u32 s91, s88, 2\n"
    "v_lshl_add_u32 v220, v244, 4, v221\n"
    "s_and_b32 s92, s91, 3\n"
    "v_xor_b32 v244, s92, v246\n"
    "v_lshl_add_u32 v244, v244, 5, v245\n"
    "v_lshl_add_u32 v244, v243, 3, v244\n"
    "v_mul_u32_u24 v245, 384, v243\n"
    "v_bfe_u32 v242, v225, 1, 2\n"
    "v_xor_b32 v243, v242, v226\n"
    "s_lshr_b32 s91, s91, 2\n"
    "v_lshlrev_b32 v243, 4, v243\n"
    "s_lshr_b32 s89, s77, 1\n"
    "s_lshl_b32 s91, s91, 10\n"
    "v_lshl_add_u32 v243, v242, 8, v243\n"
    "s_mul_i32 s80, s89, 12288\n"
    "v_add_u32 v223, s91, v244\n"
    "v_and_b32 v242, 1, v225\n"
    "v_lshrrev_b32 v244, 2, v225\n"
    "v_lshl_add_u32 v243, v242, 10, v243\n"
    "s_mov_b32 s84, s4\n"
    "s_and_b32 s85, s5, 0xffff\n"
    "s_mov_b32 s86, 73728\n"
    "s_mov_b32 s87, 0x20000\n"
    "v_mul_u32_u24 v244, 86016, v244\n"
    "s_mul_i32 s91, s77, 96\n"
    "v_lshrrev_b32 v242, 3, v225\n"
    "v_add3_u32 v244, v244, v245, s91\n"
    "v_lshl_add_u32 v243, v242, 11, v243\n"
    "v_lshl_add_u32 v10, v226, 4, v244\n"
    "v_lshl_add_u32 v11, v226, 3, v244\n"
    "v_lshlrev_b32 v242, 2, v226\n"
    "v_xor_b32 v244, v224, v226\n"
    "v_sub_u32 v242, v225, v242\n"
    "v_add_u32 v2, 65536, v229\n"
    "s_and_b32 s90, s77, 1\n"
    "s_lshl_b32 s91, s89, 10\n"
    "s_lshl_b32 s92, s90, 3\n"
    "s_add_u32 s91, s91, s92\n"
    "s_add_u32 s91, s91, 4096\n"
    "v_add_u32 v5, s91, v2\n"
    "s_lshl_b32 s91, s77, 14\n"
    "s_add_u32 s91, s91, 98304\n"
    "v_lshl_add_u32 v3, v244, 4, s91\n"
    "s_lshl_b32 s91, s89, 13\n"
    "s_lshl_b32 s92, s90, 7\n"
    "s_add_u32 s91, s91, s92\n"
    "s_add_u32 s91, s91, 98304\n"
    "v_add_u32 v219, s91, v243\n"
    "s_mov_b32 s75, 0x3e13cd3a\n"
    "v_and_b32 v243, 1, v242\n"
    "v_lshrrev_b32 v242, 1, v242\n"
    "v_cmp_eq_u32 vcc, 0, v242\n"
    "v_lshlrev_b32 v243, 4, v243\n"
    "v_mov_b32 v244, 0x3f80\n"
    "v_lshlrev_b32 v243, v243, v244\n"
    "v_cndmask_b32 v142, 0, v243, vcc\n"
    "v_cmp_eq_u32 vcc, 1, v242\n"
    "v_mov_b32 v148, v142\n"
    "s_mov_b32 s76, 0\n"
    "v_cndmask_b32 v143, 0, v243, vcc\n"
    "v_mov_b32 v149, v143\n"
    "s_add_u32 s88, s14, 256\n"
    "s_cmp_lt_u32 s88, 784\n"
    "s_cselect_b32 s69, 19267584, 0\n"
    "s_addc_u32 s76, s76, 0\n"
    "s_mov_b32 s18, s69\n"
    "s_min_u32 s26, s69, 6422528\n"
    "s_min_u32 s88, s88, 783\n"
    "s_mul_hi_u32 s89, s88, 0x924924a\n"
    "s_mul_i32 s90, s89, 28\n"
    "s_sub_u32 s90, s88, s90\n"
    "s_mul_i32 s91, s89, 688128\n"
    "s_mul_i32 s93, s90, 3072\n"
    "s_add_u32 s92, s91, 688128\n"
    "s_add_u32 s94, s93, 3072\n"
    "s_cmp_eq_u32 s89, 27\n"
    "s_cselect_b32 s92, 0, s92\n"
    "s_cmp_eq_u32 s90, 27\n"
    "s_cselect_b32 s94, 0, s94\n"
    "s_add_u32 s91, s91, 344064\n"
    "s_add_u32 s93, s93, 1536\n"
    "s_cmp_ge_u32 s77, 2\n"
    "s_cselect_b32 s88, s92, s91\n"
    "s_bitcmp1_b32 s77, 0\n"
    "s_cselect_b32 s89, s94, s93\n"
    "s_add_u32 s65, s88, s89\n"
    "s_add_u32 s72, s72, 2097152\n"
    "s_mov_b32 s74, 0xbfb8aa3b\n"
    "s_mov_b32 s73, 0x3fb8aa3b\n"
    "v_mov_b32 v12, 0x3f803f80\n"
    "v_mov_b32 v13, 0x3f803f80\n"
    "v_mov_b32 v14, 0x3f803f80\n"
    "v_mov_b32 v15, 0x3f803f80\n"
    "v_add_u32 v221, 8, v220\n"
    "v_add_u32 v220, 49152, v220\n"
    "v_add_u32 v221, 49152, v221\n"
    "v_add_u32 v223, 49152, v223\n"
    "s_add_u32 s88, s14, 512\n"
    "s_cmp_lt_u32 s88, 784\n"
    "s_cselect_b32 s70, 19267584, 0\n"
    "s_addc_u32 s76, s76, 0\n"
    "s_min_u32 s88, s88, 783\n"
    "s_mul_hi_u32 s89, s88, 0x924924a\n"
    "s_mul_i32 s90, s89, 28\n"
    "s_add_u32 s48, s91, s93\n"
    "s_add_u32 s49, s91, s94\n"
    "s_add_u32 s50, s92, s93\n"
    "s_sub_u32 s90, s88, s90\n"
    "s_add_u32 s51, s92, s94\n"
    "s_mul_i32 s91, s89, 688128\n"
    "s_mul_i32 s93, s90, 3072\n"
    "buffer_load_dwordx4 v232, s[28:31], s35 offen lds\n"
    "s_add_u32 m0, s34, 45952\n"
    "s_add_u32 s92, s91, 688128\n"
    "s_add_u32 s94, s93, 3072\n"
    "s_cmp_eq_u32 s89, 27\n"
    "s_cselect_b32 s92, 0, s92\n"
    "s_cmp_eq_u32 s90, 27\n"
    "s_cselect_b32 s94, 0, s94\n"
    "s_lshl_b32 s0, s88, 13\n"
    "s_add_u32 s91, s91, 344064\n"
    "s_add_u32 s93, s93, 1536\n"
    "s_cmp_ge_u32 s77, 2\n"
    "s_cselect_b32 s88, s92, s91\n"
    "buffer_load_dwordx4 v232, s[28:31], s35 offen offset:128 lds\n"
    "s_add_u32 m0, s34, 46848\n"
    "s_bitcmp1_b32 s77, 0\n"
    "s_cselect_b32 s89, s94, s93\n"
    "s_add_u32 s66, s88, s89\n"
    "s_lshr_b32 s89, s14, 5\n"
    "s_and_b32 s89, s89, 1\n"
    "s_and_b32 s88, s14, 7\n"
    "s_lshl_b32 s89, s89, 3\n"
    "s_add_u32 s88, s88, s89\n"
    "s_add_u32 s88, s88, 768\n"
    "s_mul_hi_u32 s89, s88, 0x924924a\n"
    "s_mul_i32 s90, s89, 28\n"
    "buffer_load_dwordx4 v232, s[28:31], s35 offen offset:256 lds\n"
    "s_add_u32 m0, s34, 151552\n"
    "s_mov_b32 s35, 36864\n"
    "s_add_u32 s52, s91, s93\n"
    "s_add_u32 s53, s91, s94\n"
    "s_add_u32 s54, s92, s93\n"
    "s_sub_u32 s90, s88, s90\n"
    "s_add_u32 s55, s92, s94\n"
    "s_mul_i32 s91, s89, 688128\n"
    "s_mul_i32 s93, s90, 3072\n"
    "s_add_u32 s92, s91, 688128\n"
    "s_add_u32 s94, s93, 3072\n"
    "s_waitcnt vmcnt(14)\n"
    "s_barrier\n"
    "buffer_load_dwordx4 v232, s[28:31], s35 offen lds\n"
    "s_add_u32 m0, s34, 152448\n"
    "v_lshl_add_u32 v40, v40, 3, s83\n"
    "v_lshl_add_u32 v41, v41, 3, s83\n"
    "ds_read_b64 v[184:185], v40\n"
    "ds_read_b64 v[186:187], v41\n"
    "v_lshl_add_u32 v42, v42, 3, s83\n"
    "v_lshl_add_u32 v43, v43, 3, s83\n"
    "ds_read_b64 v[188:189], v42\n"
    "ds_read_b64 v[190:191], v43\n"
    "v_lshl_add_u32 v44, v44, 3, s83\n"
    "v_lshl_add_u32 v45, v45, 3, s83\n"
    "ds_read_b64 v[192:193], v44\n"
    "buffer_load_dwordx4 v232, s[28:31], s35 offen offset:128 lds\n"
    "s_add_u32 m0, s34, 153344\n"
    "ds_read_b64 v[194:195], v45\n"
    "v_lshl_add_u32 v46, v46, 3, s83\n"
    "v_lshl_add_u32 v47, v47, 3, s83\n"
    "ds_read_b64 v[196:197], v46\n"
    "ds_read_b64 v[198:199], v47\n"
    "v_lshl_add_u32 v48, v48, 3, s83\n"
    "v_lshl_add_u32 v49, v49, 3, s83\n"
    "ds_read_b64 v[200:201], v48\n"
    "ds_read_b64 v[202:203], v49\n"
    "v_lshl_add_u32 v50, v50, 3, s83\n"
    "v_lshl_add_u32 v51, v51, 3, s83\n"
    "buffer_load_dwordx4 v232, s[28:31], s35 offen offset:256 lds\n"
    "s_add_u32 m0, s34, 139264\n"
    "s_mov_b32 s35, 37248\n"
    "ds_read_b64 v[204:205], v50\n"
    "ds_read_b64 v[206:207], v51\n"
    "s_waitcnt lgkmcnt(7)\n"
    "v_lshlrev_b32 v76, 16, v184\n"
    "v_lshlrev_b32 v77, 16, v186\n"
    "v_and_b32 v92, 0xffff0000, v184\n"
    "v_and_b32 v93, 0xffff0000, v186\n"
    "v_lshlrev_b32 v108, 16, v185\n"
    "v_lshlrev_b32 v109, 16, v187\n"
    "v_and_b32 v124, 0xffff0000, v185\n"
    "v_and_b32 v125, 0xffff0000, v187\n"
    "buffer_load_dwordx4 v232, s[28:31], s35 offen lds\n"
    "s_add_u32 m0, s34, 140160\n"
    "v_lshl_add_u32 v52, v52, 3, s83\n"
    "v_lshl_add_u32 v53, v53, 3, s83\n"
    "ds_read_b64 v[208:209], v52\n"
    "ds_read_b64 v[210:211], v53\n"
    "v_lshlrev_b32 v78, 16, v188\n"
    "v_lshlrev_b32 v79, 16, v190\n"
    "v_and_b32 v94, 0xffff0000, v188\n"
    "v_and_b32 v95, 0xffff0000, v190\n"
    "v_lshlrev_b32 v110, 16, v189\n"
    "v_lshlrev_b32 v111, 16, v191\n"
    "v_and_b32 v126, 0xffff0000, v189\n"
    "buffer_load_dwordx4 v232, s[28:31], s35 offen offset:128 lds\n"
    "s_add_u32 m0, s34, 141056\n"
    "v_and_b32 v127, 0xffff0000, v191\n"
    "v_lshl_add_u32 v54, v54, 3, s83\n"
    "v_lshl_add_u32 v55, v55, 3, s83\n"
    "ds_read_b64 v[212:213], v54\n"
    "ds_read_b64 v[214:215], v55\n"
    "v_lshlrev_b32 v80, 16, v192\n"
    "s_waitcnt lgkmcnt(4)\n"
    "v_lshlrev_b32 v81, 16, v194\n"
    "v_and_b32 v96, 0xffff0000, v192\n"
    "v_and_b32 v97, 0xffff0000, v194\n"
    "v_lshlrev_b32 v112, 16, v193\n"
    "v_lshlrev_b32 v113, 16, v195\n"
    "buffer_load_dwordx4 v232, s[28:31], s35 offen offset:256 lds\n"
    "s_add_u32 m0, s34, 126976\n"
    "s_mov_b32 s35, 73728\n"
    "v_and_b32 v128, 0xffff0000, v193\n"
    "v_and_b32 v129, 0xffff0000, v195\n"
    "v_lshlrev_b32 v82, 16, v196\n"
    "v_lshlrev_b32 v83, 16, v198\n"
    "v_and_b32 v98, 0xffff0000, v196\n"
    "v_and_b32 v99, 0xffff0000, v198\n"
    "v_lshlrev_b32 v114, 16, v197\n"
    "v_lshlrev_b32 v115, 16, v199\n"
    "v_and_b32 v130, 0xffff0000, v197\n"
    "v_and_b32 v131, 0xffff0000, v199\n"
    "buffer_load_dwordx4 v232, s[28:31], s35 offen lds\n"
    "s_add_u32 m0, s34, 127872\n"
    "v_lshlrev_b32 v84, 16, v200\n"
    "v_lshlrev_b32 v85, 16, v202\n"
    "v_and_b32 v100, 0xffff0000, v200\n"
    "v_and_b32 v101, 0xffff0000, v202\n"
    "v_lshlrev_b32 v116, 16, v201\n"
    "v_lshlrev_b32 v117, 16, v203\n"
    "v_and_b32 v132, 0xffff0000, v201\n"
    "v_and_b32 v133, 0xffff0000, v203\n"
    "v_lshlrev_b32 v86, 16, v204\n"
    "v_lshlrev_b32 v87, 16, v206\n"
    "v_and_b32 v102, 0xffff0000, v204\n"
    "buffer_load_dwordx4 v232, s[28:31], s35 offen offset:128 lds\n"
    "s_add_u32 m0, s34, 128768\n"
    "v_and_b32 v103, 0xffff0000, v206\n"
    "v_lshlrev_b32 v118, 16, v205\n"
    "v_lshlrev_b32 v119, 16, v207\n"
    "v_and_b32 v134, 0xffff0000, v205\n"
    "v_and_b32 v135, 0xffff0000, v207\n"
    "s_waitcnt lgkmcnt(0)\n"
    "v_lshlrev_b32 v88, 16, v208\n"
    "v_lshlrev_b32 v89, 16, v210\n"
    "v_and_b32 v104, 0xffff0000, v208\n"
    "v_and_b32 v105, 0xffff0000, v210\n"
    "v_lshlrev_b32 v120, 16, v209\n"
    "v_lshlrev_b32 v121, 16, v211\n"
    "buffer_load_dwordx4 v232, s[28:31], s35 offen offset:256 lds\n"
    "s_add_u32 m0, s34, 114688\n"
    "s_mov_b32 s35, 74112\n"
    "v_and_b32 v136, 0xffff0000, v209\n"
    "v_and_b32 v137, 0xffff0000, v211\n"
    "v_lshlrev_b32 v90, 16, v212\n"
    "v_lshlrev_b32 v91, 16, v214\n"
    "v_and_b32 v106, 0xffff0000, v212\n"
    "v_and_b32 v107, 0xffff0000, v214\n"
    "v_lshlrev_b32 v122, 16, v213\n"
    "v_lshlrev_b32 v123, 16, v215\n"
    "v_and_b32 v138, 0xffff0000, v213\n"
    "v_and_b32 v139, 0xffff0000, v215\n"
    "buffer_load_dwordx4 v232, s[28:31], s35 offen lds\n"
    "s_add_u32 m0, s34, 115584\n"
    "s_cmp_eq_u32 s89, 27\n"
    "s_cselect_b32 s92, 0, s92\n"
    "s_cmp_eq_u32 s90, 27\n"
    "s_cselect_b32 s94, 0, s94\n"
    "s_lshl_b32 s1, s88, 13\n"
    "s_add_u32 s91, s91, 344064\n"
    "s_add_u32 s93, s93, 1536\n"
    "s_cmp_lt_u32 s14, 64\n"
    "s_cselect_b32 s71, 19267584, 0\n"
    "s_cmp_ge_u32 s77, 2\n"
    "s_cselect_b32 s88, s92, s91\n"
    "buffer_load_dwordx4 v232, s[28:31], s35 offen offset:128 lds\n"
    "s_add_u32 m0, s34, 116480\n"
    "s_bitcmp1_b32 s77, 0\n"
    "s_cselect_b32 s89, s94, s93\n"
    "s_mov_b32 s69, s70\n"
    "s_lshl_b32 s82, s77, 10\n"
    "s_add_u32 s67, s88, s89\n"
    "s_lshr_b32 s15, s14, 3\n"
    "s_mov_b32 s70, s71\n"
    "v_add_u32 v11, 64, v11\n"
    "v_add_u32 v4, s82, v2\n"
    "s_mov_b32 s2, 0xff7fff7f\n"
    "s_mov_b32 s20, s12\n"
    "buffer_load_dwordx4 v232, s[28:31], s35 offen offset:256 lds\n"
    "s_add_u32 m0, s34, 102400\n"
    "s_mov_b32 s35, 110592\n"
    "s_and_b32 s21, s13, 0xffff\n"
    "s_mov_b32 s22, 19267584\n"
    "s_mov_b32 s23, 0x20000\n"
    "s_add_u32 s56, s91, s93\n"
    "s_add_u32 s57, s91, s94\n"
    "s_add_u32 s58, s92, s93\n"
    "s_add_u32 s59, s92, s94\n"
    "s_and_b32 s15, s15, 3\n"
    "s_mov_b32 s71, 0\n"
    "s_mov_b32 s78, s33\n"
    "buffer_load_dwordx4 v232, s[28:31], s35 offen lds\n"
    "s_add_u32 m0, s34, 103296\n"
    "s_nop 0\n"
    "buffer_load_dwordx4 v232, s[28:31], s35 offen offset:128 lds\n"
    "s_add_u32 m0, s34, 104192\n"
    "s_nop 0\n"
    "buffer_load_dwordx4 v232, s[28:31], s35 offen offset:256 lds\n"
    "s_add_u32 m0, s34, 57344\n"
    "s_mov_b32 s35, 110976\n"
    "s_waitcnt vmcnt(15)\n"
    "s_barrier\n"
    "buffer_load_dwordx4 v232, s[28:31], s35 offen lds\n"
    "s_add_u32 m0, s34, 58240\n"
    "ds_read_b64_tr_b16 a[0:1], v216 offset:0\n"
    "ds_read_b64_tr_b16 a[2:3], v216 offset:512\n"
    "ds_read_b128 v[16:19], v0 offset:0\n"
    "ds_read_b64_tr_b16 a[24:25], v217 offset:0\n"
    "ds_read_b64_tr_b16 a[26:27], v217 offset:512\n"
    "ds_read_b64_tr_b16 a[48:49], v218 offset:0\n"
    "ds_read_b64_tr_b16 a[50:51], v218 offset:512\n"
    "ds_read_b64_tr_b16 a[72:73], v216 offset:12288\n"
    "ds_read_b64_tr_b16 a[74:75], v216 offset:12800\n"
    "ds_read_b64_tr_b16 a[96:97], v217 offset:12288\n"
    "buffer_load_dwordx4 v232, s[28:31], s35 offen offset:128 lds\n"
    "s_add_u32 m0, s34, 59136\n"
    "ds_read_b64_tr_b16 a[98:99], v217 offset:12800\n"
    "ds_read_b64_tr_b16 a[120:121], v218 offset:12288\n"
    "ds_read_b64_tr_b16 a[122:123], v218 offset:12800\n"
    "ds_read_b128 v[20:23], v0 offset:6144\n"
    "ds_read_b128 v[24:27], v0 offset:12288\n"
    "ds_read_b128 v[28:31], v0 offset:18432\n"
    "ds_read_b128 v[32:35], v1 offset:0\n"
    "buffer_load_dwordx4 v232, s[28:31], s35 offen offset:256 lds\n"
    "s_add_u32 m0, s34, 69632\n"
    "s_waitcnt lgkmcnt(12)\n"
    "v_mfma_f32_16x16x32_bf16 v[152:155], a[0:3], v[16:19], 0\n"
    "s_mov_b32 s35, 147456\n"
    "ds_read_b128 v[36:39], v1 offset:6144\n"
    "v_mfma_f32_16x16x32_bf16 v[156:159], a[24:27], v[16:19], 0\n"
    "s_waitcnt lgkmcnt(8)\n"
    "v_mfma_f32_16x16x32_bf16 v[160:163], a[48:51], v[16:19], 0\n"
    "v_mfma_f32_16x16x32_bf16 v[164:167], a[72:75], v[16:19], 0\n"
    "buffer_load_dwordx4 v232, s[28:31], s35 offen lds\n"
    "s_waitcnt lgkmcnt(1)\n"
    "v_mfma_f32_16x16x32_bf16 v[168:171], a[96:99], v[16:19], 0\n"
    "s_add_u32 m0, s34, 70528\n"
    "v_mfma_f32_16x16x32_bf16 v[172:175], a[120:123], v[16:19], 0\n"
    "ds_read_b128 v[16:19], v1 offset:12288\n"
    "v_mfma_f32_16x16x32_bf16 v[176:179], a[0:3], v[20:23], 0\n"
    "v_mfma_f32_16x16x32_bf16 v[180:183], a[24:27], v[20:23], 0\n"
    "buffer_load_dwordx4 v232, s[28:31], s35 offen offset:128 lds\n"
    "v_mfma_f32_16x16x32_bf16 v[184:187], a[48:51], v[20:23], 0\n"
    "s_add_u32 m0, s34, 71424\n"
    "v_mfma_f32_16x16x32_bf16 v[188:191], a[72:75], v[20:23], 0\n"
    "v_mfma_f32_16x16x32_bf16 v[192:195], a[96:99], v[20:23], 0\n"
    "v_mfma_f32_16x16x32_bf16 v[196:199], a[120:123], v[20:23], 0\n"
    "buffer_load_dwordx4 v232, s[28:31], s35 offen offset:256 lds\n"
    "v_mfma_f32_16x16x32_bf16 v[200:203], a[0:3], v[24:27], 0\n"
    "s_add_u32 m0, s34, 81920\n"
    "s_mov_b32 s35, 147840\n"
    "v_mfma_f32_16x16x32_bf16 v[204:207], a[24:27], v[24:27], 0\n"
    "s_waitcnt vmcnt(15)\n"
    "s_barrier\n"
    "v_mfma_f32_16x16x32_bf16 v[208:211], a[48:51], v[24:27], 0\n"
    "buffer_load_dwordx4 v232, s[28:31], s35 offen lds\n"
    "v_mfma_f32_16x16x32_bf16 v[212:215], a[72:75], v[24:27], 0\n"
    "s_add_u32 m0, s34, 82816\n"
    "ds_read_b64_tr_b16 a[4:5], v248 offset:49152\n"
    "v_mfma_f32_16x16x32_bf16 v[40:43], a[96:99], v[24:27], 0\n"
    "ds_read_b64_tr_b16 a[6:7], v248 offset:49664\n"
    "ds_read_b64_tr_b16 a[28:29], v249 offset:49152\n"
    "v_mfma_f32_16x16x32_bf16 v[44:47], a[120:123], v[24:27], 0\n"
    "ds_read_b64_tr_b16 a[30:31], v249 offset:49664\n"
    "ds_read_b64_tr_b16 a[52:53], v250 offset:49152\n"
    "v_mfma_f32_16x16x32_bf16 v[48:51], a[0:3], v[28:31], 0\n"
    "buffer_load_dwordx4 v232, s[28:31], s35 offen offset:128 lds\n"
    "v_mfma_f32_16x16x32_bf16 v[52:55], a[24:27], v[28:31], 0\n"
    "s_add_u32 m0, s34, 83712\n"
    "ds_read_b64_tr_b16 a[54:55], v250 offset:49664\n"
    "v_mfma_f32_16x16x32_bf16 v[56:59], a[48:51], v[28:31], 0\n"
    "ds_read_b64_tr_b16 a[76:77], v248 offset:36864\n"
    "ds_read_b64_tr_b16 a[78:79], v248 offset:37376\n"
    "v_mfma_f32_16x16x32_bf16 v[60:63], a[72:75], v[28:31], 0\n"
    "ds_read_b64_tr_b16 a[100:101], v249 offset:36864\n"
    "ds_read_b64_tr_b16 a[102:103], v249 offset:37376\n"
    "v_mfma_f32_16x16x32_bf16 v[64:67], a[96:99], v[28:31], 0\n"
    "buffer_load_dwordx4 v232, s[28:31], s35 offen offset:256 lds\n"
    "v_mfma_f32_16x16x32_bf16 v[68:71], a[120:123], v[28:31], 0\n"
    "s_add_u32 m0, s34, 32768\n"
    "s_mov_b32 s35, 184320\n"
    "s_waitcnt lgkmcnt(5)\n"
    "v_mfma_f32_16x16x32_bf16 v[152:155], a[4:7], v[32:35], v[152:155]\n"
    "ds_read_b64_tr_b16 a[124:125], v250 offset:36864\n"
    "ds_read_b64_tr_b16 a[126:127], v250 offset:37376\n"
    "v_mfma_f32_16x16x32_bf16 v[156:159], a[28:31], v[32:35], v[156:159]\n"
    "ds_read_b128 v[20:23], v1 offset:18432\n"
    "buffer_load_dwordx4 v232, s[28:31], s35 offen lds\n"
    "s_waitcnt lgkmcnt(3)\n"
    "v_mfma_f32_16x16x32_bf16 v[160:163], a[52:55], v[32:35], v[160:163]\n"
    "s_add_u32 m0, s34, 33664\n"
    "v_mfma_f32_16x16x32_bf16 v[164:167], a[76:79], v[32:35], v[164:167]\n"
    "v_mfma_f32_16x16x32_bf16 v[168:171], a[100:103], v[32:35], v[168:171]\n"
    "buffer_load_dwordx4 v232, s[28:31], s35 offen offset:128 lds\n"
    "s_add_u32 m0, s34, 34560\n"
    "s_nop 0\n"
    "s_waitcnt lgkmcnt(1)\n"
    "v_mfma_f32_16x16x32_bf16 v[172:175], a[124:127], v[32:35], v[172:175]\n"
    "v_mfma_f32_16x16x32_bf16 v[176:179], a[4:7], v[36:39], v[176:179]\n"
    "v_mfma_f32_16x16x32_bf16 v[180:183], a[28:31], v[36:39], v[180:183]\n"
    "buffer_load_dwordx4 v232, s[28:31], s35 offen offset:256 lds\n"
    "v_mfma_f32_16x16x32_bf16 v[184:187], a[52:55], v[36:39], v[184:187]\n"
    "s_add_u32 m0, s34, 45056\n"
    "s_mov_b32 s35, 184704\n"
    "v_mfma_f32_16x16x32_bf16 v[188:191], a[76:79], v[36:39], v[188:191]\n"
    "s_waitcnt vmcnt(15)\n"
    "s_barrier\n"
    "v_mfma_f32_16x16x32_bf16 v[192:195], a[100:103], v[36:39], v[192:195]\n"
    "buffer_load_dwordx4 v232, s[28:31], s35 offen lds\n"
    "v_mfma_f32_16x16x32_bf16 v[196:199], a[124:127], v[36:39], v[196:199]\n"
    "s_add_u32 m0, s34, 45952\n"
    "ds_read_b64_tr_b16 a[8:9], v248 offset:24576\n"
    "v_mfma_f32_16x16x32_bf16 v[200:203], a[4:7], v[16:19], v[200:203]\n"
    "ds_read_b64_tr_b16 a[10:11], v248 offset:25088\n"
    "ds_read_b128 v[24:27], v0 offset:2048\n"
    "v_mfma_f32_16x16x32_bf16 v[204:207], a[28:31], v[16:19], v[204:207]\n"
    "ds_read_b64_tr_b16 a[32:33], v249 offset:24576\n"
    "ds_read_b64_tr_b16 a[34:35], v249 offset:25088\n"
    "v_mfma_f32_16x16x32_bf16 v[208:211], a[52:55], v[16:19], v[208:211]\n"
    "buffer_load_dwordx4 v232, s[28:31], s35 offen offset:128 lds\n"
    "v_mfma_f32_16x16x32_bf16 v[212:215], a[76:79], v[16:19], v[212:215]\n"
    "s_add_u32 m0, s34, 46848\n"
    "ds_read_b64_tr_b16 a[56:57], v250 offset:24576\n"
    "v_mfma_f32_16x16x32_bf16 v[40:43], a[100:103], v[16:19], v[40:43]\n"
    "ds_read_b64_tr_b16 a[58:59], v250 offset:25088\n"
    "ds_read_b64_tr_b16 a[80:81], v248 offset:12288\n"
    "v_mfma_f32_16x16x32_bf16 v[44:47], a[124:127], v[16:19], v[44:47]\n"
    "ds_read_b64_tr_b16 a[82:83], v248 offset:12800\n"
    "ds_read_b64_tr_b16 a[104:105], v249 offset:12288\n"
    "s_waitcnt lgkmcnt(6)\n"
    "v_mfma_f32_16x16x32_bf16 v[48:51], a[4:7], v[20:23], v[48:51]\n"
    "buffer_load_dwordx4 v232, s[28:31], s35 offen offset:256 lds\n"
    "v_mfma_f32_16x16x32_bf16 v[52:55], a[28:31], v[20:23], v[52:55]\n"
    "s_add_u32 s35, s80, 12288\n"
    "s_add_u32 m0, s34, 57344\n"
    "v_mfma_f32_16x16x32_bf16 v[56:59], a[52:55], v[20:23], v[56:59]\n"
    "ds_read_b64_tr_b16 a[106:107], v249 offset:12800\n"
    "ds_read_b64_tr_b16 a[128:129], v250 offset:12288\n"
    "v_mfma_f32_16x16x32_bf16 v[60:63], a[76:79], v[20:23], v[60:63]\n"
    "ds_read_b64_tr_b16 a[130:131], v250 offset:12800\n"
    "ds_read_b128 v[28:31], v0 offset:8192\n"
    "v_mfma_f32_16x16x32_bf16 v[64:67], a[100:103], v[20:23], v[64:67]\n"
    "buffer_load_dwordx4 a[232:235], v241, s[84:87], s35 offen offset:0\n"
    "v_mfma_f32_16x16x32_bf16 v[68:71], a[124:127], v[20:23], v[68:71]\n"
    "ds_read_b128 v[32:35], v0 offset:14336\n"
    "v_mfma_f32_16x16x32_bf16 v[152:155], a[8:11], v[24:27], v[152:155]\n"
    "ds_read_b128 v[36:39], v0 offset:20480\n"
    "s_waitcnt lgkmcnt(6)\n"
    "v_mfma_f32_16x16x32_bf16 v[156:159], a[32:35], v[24:27], v[156:159]\n"
    "ds_read_b128 v[16:19], v1 offset:2048\n"
    "v_mfma_f32_16x16x32_bf16 v[160:163], a[56:59], v[24:27], v[160:163]\n"
    "buffer_load_dwordx4 v[224:227], v241, s[84:87], s35 offen offset:128\n"
    "v_mfma_f32_16x16x32_bf16 v[164:167], a[80:83], v[24:27], v[164:167]\n"
    "ds_read_b128 v[20:23], v1 offset:8192\n"
    "s_waitcnt lgkmcnt(4)\n"
    "v_mfma_f32_16x16x32_bf16 v[168:171], a[104:107], v[24:27], v[168:171]\n"
    "ds_write_b128 v219, v[124:127] offset:49152\n"
    "v_mfma_f32_16x16x32_bf16 v[172:175], a[128:131], v[24:27], v[172:175]\n"
    "ds_read_b128 v[24:27], v1 offset:14336\n"
    "v_mfma_f32_16x16x32_bf16 v[176:179], a[8:11], v[28:31], v[176:179]\n"
    "ds_write_b128 v219, v[128:131] offset:53248\n"
    "v_mfma_f32_16x16x32_bf16 v[180:183], a[32:35], v[28:31], v[180:183]\n"
    "s_waitcnt vmcnt(14)\n"
    "s_barrier\n"
    "v_mfma_f32_16x16x32_bf16 v[184:187], a[56:59], v[28:31], v[184:187]\n"
    "ds_read_b64_tr_b16 a[12:13], v248 offset:0\n"
    "ds_read_b64_tr_b16 a[14:15], v248 offset:512\n"
    "v_mfma_f32_16x16x32_bf16 v[188:191], a[80:83], v[28:31], v[188:191]\n"
    "ds_read_b64_tr_b16 a[36:37], v249 offset:0\n"
    "ds_read_b64_tr_b16 a[38:39], v249 offset:512\n"
    "v_mfma_f32_16x16x32_bf16 v[192:195], a[104:107], v[28:31], v[192:195]\n"
    "ds_read_b64_tr_b16 a[60:61], v250 offset:0\n"
    "ds_read_b64_tr_b16 a[62:63], v250 offset:512\n"
    "v_mfma_f32_16x16x32_bf16 v[196:199], a[128:131], v[28:31], v[196:199]\n"
    "buffer_load_dwordx4 v[248:251], v241, s[84:87], s35 offen offset:256\n"
    "s_waitcnt lgkmcnt(6)\n"
    "v_mfma_f32_16x16x32_bf16 v[200:203], a[8:11], v[32:35], v[200:203]\n"
    "s_add_u32 s35, s80, 49152\n"
    "ds_read_b64_tr_b16 a[84:85], v216 offset:24576\n"
    "v_mfma_f32_16x16x32_bf16 v[204:207], a[32:35], v[32:35], v[204:207]\n"
    "ds_read_b64_tr_b16 a[86:87], v216 offset:25088\n"
    "ds_read_b64_tr_b16 a[108:109], v217 offset:24576\n"
    "v_mfma_f32_16x16x32_bf16 v[208:211], a[56:59], v[32:35], v[208:211]\n"
    "ds_read_b64_tr_b16 a[110:111], v217 offset:25088\n"
    "ds_read_b64_tr_b16 a[132:133], v218 offset:24576\n"
    "v_mfma_f32_16x16x32_bf16 v[212:215], a[80:83], v[32:35], v[212:215]\n"
    "buffer_load_dwordx4 a[236:239], v241, s[84:87], s35 offen offset:0\n"
    "v_mfma_f32_16x16x32_bf16 v[40:43], a[104:107], v[32:35], v[40:43]\n"
    "ds_read_b64_tr_b16 a[134:135], v218 offset:25088\n"
    "ds_read_b128 v[28:31], v1 offset:20480\n"
    "v_mfma_f32_16x16x32_bf16 v[44:47], a[128:131], v[32:35], v[44:47]\n"
    "ds_write_b128 v219, v[132:135] offset:49216\n"
    "v_mfma_f32_16x16x32_bf16 v[48:51], a[8:11], v[36:39], v[48:51]\n"
    "buffer_load_dwordx4 v[228:231], v241, s[84:87], s35 offen offset:128\n"
    "v_mfma_f32_16x16x32_bf16 v[52:55], a[32:35], v[36:39], v[52:55]\n"
    "ds_write_b128 v219, v[136:139] offset:53312\n"
    "v_mfma_f32_16x16x32_bf16 v[56:59], a[56:59], v[36:39], v[56:59]\n"
    "buffer_load_dwordx4 v[252:255], v241, s[84:87], s35 offen offset:256\n"
    "v_mfma_f32_16x16x32_bf16 v[60:63], a[80:83], v[36:39], v[60:63]\n"
    "s_mov_b32 s35, 768\n"
    "v_mfma_f32_16x16x32_bf16 v[64:67], a[104:107], v[36:39], v[64:67]\n"
    "ds_write_b128 v219, v[108:111] offset:32768\n"
    "v_mfma_f32_16x16x32_bf16 v[68:71], a[128:131], v[36:39], v[68:71]\n"
    "s_waitcnt lgkmcnt(1)\n"
    "v_mfma_f32_16x16x32_bf16 v[152:155], a[12:15], v[16:19], v[152:155]\n"
    "ds_write_b128 v219, v[112:115] offset:36864\n"
    "v_mfma_f32_16x16x32_bf16 v[156:159], a[36:39], v[16:19], v[156:159]\n"
    "v_mfma_f32_16x16x32_bf16 v[160:163], a[60:63], v[16:19], v[160:163]\n"
    "ds_write_b128 v219, v[116:119] offset:32832\n"
    "v_mfma_f32_16x16x32_bf16 v[164:167], a[84:87], v[16:19], v[164:167]\n"
    "s_waitcnt vmcnt(12)\n"
    "s_barrier\n"
    "v_mfma_f32_16x16x32_bf16 v[168:171], a[108:111], v[16:19], v[168:171]\n"
    "buffer_load_dwordx4 v232, s[28:31], s35 offen lds\n"
    "v_mfma_f32_16x16x32_bf16 v[172:175], a[132:135], v[16:19], v[172:175]\n"
    "s_add_u32 m0, s34, 58240\n"
    "ds_read_b64_tr_b16 a[16:17], v216 offset:36864\n"
    "v_mfma_f32_16x16x32_bf16 v[176:179], a[12:15], v[20:23], v[176:179]\n"
    "ds_write_b128 v219, v[120:123] offset:36928\n"
    "v_mfma_f32_16x16x32_bf16 v[180:183], a[36:39], v[20:23], v[180:183]\n"
    "buffer_load_dwordx4 v232, s[28:31], s35 offen offset:128 lds\n"
    "v_mfma_f32_16x16x32_bf16 v[184:187], a[60:63], v[20:23], v[184:187]\n"
    "s_add_u32 m0, s34, 59136\n"
    "ds_read_b64_tr_b16 a[18:19], v216 offset:37376\n"
    "v_mfma_f32_16x16x32_bf16 v[188:191], a[84:87], v[20:23], v[188:191]\n"
    "ds_write_b128 v219, v[92:95] offset:16384\n"
    "v_mfma_f32_16x16x32_bf16 v[192:195], a[108:111], v[20:23], v[192:195]\n"
    "buffer_load_dwordx4 v232, s[28:31], s35 offen offset:256 lds\n"
    "v_mfma_f32_16x16x32_bf16 v[196:199], a[132:135], v[20:23], v[196:199]\n"
    "s_add_u32 m0, s34, 69632\n"
    "s_mov_b32 s35, 37632\n"
    "v_mfma_f32_16x16x32_bf16 v[200:203], a[12:15], v[24:27], v[200:203]\n"
    "ds_write_b128 v219, v[96:99] offset:20480\n"
    "v_mfma_f32_16x16x32_bf16 v[204:207], a[36:39], v[24:27], v[204:207]\n"
    "ds_read_b128 v[32:35], v0 offset:4096\n"
    "v_mfma_f32_16x16x32_bf16 v[208:211], a[60:63], v[24:27], v[208:211]\n"
    "ds_write_b128 v219, v[100:103] offset:16448\n"
    "v_mfma_f32_16x16x32_bf16 v[212:215], a[84:87], v[24:27], v[212:215]\n"
    "ds_read_b64_tr_b16 a[40:41], v217 offset:36864\n"
    "ds_read_b64_tr_b16 a[42:43], v217 offset:37376\n"
    "v_mfma_f32_16x16x32_bf16 v[40:43], a[108:111], v[24:27], v[40:43]\n"
    "ds_write_b128 v219, v[104:107] offset:20544\n"
    "v_mfma_f32_16x16x32_bf16 v[44:47], a[132:135], v[24:27], v[44:47]\n"
    "ds_read_b64_tr_b16 a[64:65], v218 offset:36864\n"
    "ds_read_b64_tr_b16 a[66:67], v218 offset:37376\n"
    "v_mfma_f32_16x16x32_bf16 v[48:51], a[12:15], v[28:31], v[48:51]\n"
    "ds_write_b128 v219, v[76:79] offset:0\n"
    "v_mfma_f32_16x16x32_bf16 v[52:55], a[36:39], v[28:31], v[52:55]\n"
    "ds_read_b64_tr_b16 a[88:89], v216 offset:49152\n"
    "ds_read_b64_tr_b16 a[90:91], v216 offset:49664\n"
    "v_mfma_f32_16x16x32_bf16 v[56:59], a[60:63], v[28:31], v[56:59]\n"
    "ds_write_b128 v219, v[80:83] offset:4096\n"
    "v_mfma_f32_16x16x32_bf16 v[60:63], a[84:87], v[28:31], v[60:63]\n"
    "ds_read_b64_tr_b16 a[112:113], v217 offset:49152\n"
    "ds_read_b64_tr_b16 a[114:115], v217 offset:49664\n"
    "v_mfma_f32_16x16x32_bf16 v[64:67], a[108:111], v[28:31], v[64:67]\n"
    "ds_write_b128 v219, v[84:87] offset:64\n"
    "v_mfma_f32_16x16x32_bf16 v[68:71], a[132:135], v[28:31], v[68:71]\n"
    "ds_read_b64_tr_b16 a[136:137], v218 offset:49152\n"
    "ds_read_b64_tr_b16 a[138:139], v218 offset:49664\n"
    "s_waitcnt lgkmcnt(5)\n"
    "v_mfma_f32_16x16x32_bf16 v[152:155], a[16:19], v[32:35], v[152:155]\n"
    "ds_write_b128 v219, v[88:91] offset:4160\n"
    "v_mfma_f32_16x16x32_bf16 v[156:159], a[40:43], v[32:35], v[156:159]\n"
    "ds_read_b128 v[36:39], v0 offset:10240\n"
    "v_mfma_f32_16x16x32_bf16 v[160:163], a[64:67], v[32:35], v[160:163]\n"
    "ds_read_b128 v[16:19], v0 offset:16384\n"
    "v_mfma_f32_16x16x32_bf16 v[164:167], a[88:91], v[32:35], v[164:167]\n"
    "ds_read_b128 v[20:23], v0 offset:22528\n"
    "s_waitcnt lgkmcnt(4)\n"
    "v_mfma_f32_16x16x32_bf16 v[168:171], a[112:115], v[32:35], v[168:171]\n"
    "ds_read_b128 v[24:27], v1 offset:4096\n"
    "ds_read_b128 v[28:31], v1 offset:10240\n"
    "v_mfma_f32_16x16x32_bf16 v[172:175], a[136:139], v[32:35], v[172:175]\n"
    "s_waitcnt vmcnt(9)\n"
    "s_barrier\n"
    "buffer_load_dwordx4 v232, s[28:31], s35 offen lds\n"
    "s_waitcnt lgkmcnt(3)\n"
    "v_mfma_f32_16x16x32_bf16 v[176:179], a[16:19], v[36:39], v[176:179]\n"
    "s_add_u32 m0, s34, 70528\n"
    "ds_read_b64_tr_b16 a[20:21], v216 offset:0\n"
    "v_mfma_f32_16x16x32_bf16 v[180:183], a[40:43], v[36:39], v[180:183]\n"
    "ds_read_b64_tr_b16 a[22:23], v216 offset:512\n"
    "ds_read_b64_tr_b16 a[44:45], v217 offset:0\n"
    "v_mfma_f32_16x16x32_bf16 v[184:187], a[64:67], v[36:39], v[184:187]\n"
    "ds_read_b64_tr_b16 a[46:47], v217 offset:512\n"
    "ds_read_b64_tr_b16 a[68:69], v218 offset:0\n"
    "v_mfma_f32_16x16x32_bf16 v[188:191], a[88:91], v[36:39], v[188:191]\n"
    "buffer_load_dwordx4 v232, s[28:31], s35 offen offset:128 lds\n"
    "v_mfma_f32_16x16x32_bf16 v[192:195], a[112:115], v[36:39], v[192:195]\n"
    "s_add_u32 m0, s34, 71424\n"
    "ds_read_b64_tr_b16 a[70:71], v218 offset:512\n"
    "v_mfma_f32_16x16x32_bf16 v[196:199], a[136:139], v[36:39], v[196:199]\n"
    "ds_read_b64_tr_b16 a[92:93], v216 offset:12288\n"
    "ds_read_b64_tr_b16 a[94:95], v216 offset:12800\n"
    "v_mfma_f32_16x16x32_bf16 v[200:203], a[16:19], v[16:19], v[200:203]\n"
    "ds_read_b64_tr_b16 a[116:117], v217 offset:12288\n"
    "ds_read_b64_tr_b16 a[118:119], v217 offset:12800\n"
    "v_mfma_f32_16x16x32_bf16 v[204:207], a[40:43], v[16:19], v[204:207]\n"
    "buffer_load_dwordx4 v232, s[28:31], s35 offen offset:256 lds\n"
    "v_mfma_f32_16x16x32_bf16 v[208:211], a[64:67], v[16:19], v[208:211]\n"
    "s_add_u32 m0, s34, 81920\n"
    "s_mov_b32 s35, 74496\n"
    "v_mfma_f32_16x16x32_bf16 v[212:215], a[88:91], v[16:19], v[212:215]\n"
    "ds_read_b64_tr_b16 a[140:141], v218 offset:12288\n"
    "ds_read_b64_tr_b16 a[142:143], v218 offset:12800\n"
    "v_mfma_f32_16x16x32_bf16 v[40:43], a[112:115], v[16:19], v[40:43]\n"
    "ds_read_b128 v[32:35], v1 offset:16384\n"
    "v_mfma_f32_16x16x32_bf16 v[44:47], a[136:139], v[16:19], v[44:47]\n"
    "buffer_load_dwordx4 v232, s[28:31], s35 offen lds\n"
    "s_waitcnt lgkmcnt(3)\n"
    "v_mfma_f32_16x16x32_bf16 v[48:51], a[16:19], v[20:23], v[48:51]\n"
    "s_add_u32 m0, s34, 82816\n"
    "ds_read_b128 v[36:39], v1 offset:22528\n"
    "v_mfma_f32_16x16x32_bf16 v[52:55], a[40:43], v[20:23], v[52:55]\n"
    "ds_read_b128 v[16:19], v0 offset:0\n"
    "v_mfma_f32_16x16x32_bf16 v[56:59], a[64:67], v[20:23], v[56:59]\n"
    "v_mfma_f32_16x16x32_bf16 v[60:63], a[88:91], v[20:23], v[60:63]\n"
    "buffer_load_dwordx4 v232, s[28:31], s35 offen offset:128 lds\n"
    "v_mfma_f32_16x16x32_bf16 v[64:67], a[112:115], v[20:23], v[64:67]\n"
    "s_add_u32 m0, s34, 83712\n"
    "v_mfma_f32_16x16x32_bf16 v[68:71], a[136:139], v[20:23], v[68:71]\n"
    "ds_read_b128 v[20:23], v0 offset:6144\n"
    "v_mfma_f32_16x16x32_bf16 v[152:155], a[20:23], v[24:27], v[152:155]\n"
    "v_mfma_f32_16x16x32_bf16 v[156:159], a[44:47], v[24:27], v[156:159]\n"
    "buffer_load_dwordx4 v232, s[28:31], s35 offen offset:256 lds\n"
    "v_mfma_f32_16x16x32_bf16 v[160:163], a[68:71], v[24:27], v[160:163]\n"
    "s_add_u32 m0, s34, 32768\n"
    "s_mov_b32 s35, 111360\n"
    "v_mfma_f32_16x16x32_bf16 v[164:167], a[92:95], v[24:27], v[164:167]\n"
    "v_mfma_f32_16x16x32_bf16 v[168:171], a[116:119], v[24:27], v[168:171]\n"
    "s_nop 0\n"
    "v_mul_f32 v152, s75, v152\n"
    "s_waitcnt lgkmcnt(0)\n"
    "v_mfma_f32_16x16x32_bf16 v[172:175], a[140:143], v[24:27], v[172:175]\n"
    "v_mul_f32 v153, s75, v153\n"
    "v_mul_f32 v154, s75, v154\n"
    "v_mfma_f32_16x16x32_bf16 v[176:179], a[20:23], v[28:31], v[176:179]\n"
    "v_mul_f32 v155, s75, v155\n"
    "v_cvt_pk_bf16_f32 v76, v152, v153\n"
    "v_mfma_f32_16x16x32_bf16 v[180:183], a[44:47], v[28:31], v[180:183]\n"
    "v_cvt_pk_bf16_f32 v77, v154, v155\n"
    "v_mul_f32 v156, s75, v156\n"
    "v_mfma_f32_16x16x32_bf16 v[184:187], a[68:71], v[28:31], v[184:187]\n"
    "v_mul_f32 v157, s75, v157\n"
    "v_mul_f32 v158, s75, v158\n"
    "v_mfma_f32_16x16x32_bf16 v[188:191], a[92:95], v[28:31], v[188:191]\n"
    "s_waitcnt vmcnt(6)\n"
    "s_barrier\n"
    "v_mfma_f32_16x16x32_bf16 v[192:195], a[116:119], v[28:31], v[192:195]\n"
    "buffer_load_dwordx4 v232, s[28:31], s35 offen lds\n"
    "v_mfma_f32_16x16x32_bf16 v[196:199], a[140:143], v[28:31], v[196:199]\n"
    "s_add_u32 m0, s34, 33664\n"
    "ds_read_b128 v[152:155], v3 offset:0\n"
    "v_mfma_f32_16x16x32_bf16 v[200:203], a[20:23], v[32:35], v[200:203]\n"
    "ds_read_b128 v[94:97], v0 offset:24576\n"
    "v_mul_f32 v159, s75, v159\n"
    "v_mfma_f32_16x16x32_bf16 v[204:207], a[44:47], v[32:35], v[204:207]\n"
    "v_cvt_pk_bf16_f32 v78, v156, v157\n"
    "v_cvt_pk_bf16_f32 v79, v158, v159\n"
    "v_mfma_f32_16x16x32_bf16 v[208:211], a[68:71], v[32:35], v[208:211]\n"
    "buffer_load_dwordx4 v232, s[28:31], s35 offen offset:128 lds\n"
    "v_mfma_f32_16x16x32_bf16 v[212:215], a[92:95], v[32:35], v[212:215]\n"
    "s_add_u32 m0, s34, 34560\n"
    "ds_read_b128 v[156:159], v3 offset:1024\n"
    "v_mfma_f32_16x16x32_bf16 v[40:43], a[116:119], v[32:35], v[40:43]\n"
    "v_cvt_pk_bf16_f32 v126, v168, v169\n"
    "v_cvt_pk_bf16_f32 v127, v170, v171\n"
    "v_mfma_f32_16x16x32_bf16 v[44:47], a[140:143], v[32:35], v[44:47]\n"
    "ds_read_b128 v[168:171], v3 offset:4096\n"
    "v_mul_f32 v160, s75, v160\n"
    "v_mfma_f32_16x16x32_bf16 v[48:51], a[20:23], v[36:39], v[48:51]\n"
    "buffer_load_dwordx4 v232, s[28:31], s35 offen offset:256 lds\n"
    "v_mfma_f32_16x16x32_bf16 v[52:55], a[44:47], v[36:39], v[52:55]\n"
    "s_add_u32 m0, s34, 45056\n"
    "s_mov_b32 s35, 148224\n"
    "v_mfma_f32_16x16x32_bf16 v[56:59], a[68:71], v[36:39], v[56:59]\n"
    "ds_read_b128 v[102:105], v0 offset:26624\n"
    "v_mul_f32 v161, s75, v161\n"
    "v_mfma_f32_16x16x32_bf16 v[60:63], a[92:95], v[36:39], v[60:63]\n"
    "v_mul_f32 v162, s75, v162\n"
    "v_mul_f32 v163, s75, v163\n"
    "v_mfma_f32_16x16x32_bf16 v[64:67], a[116:119], v[36:39], v[64:67]\n"
    "buffer_load_dwordx4 v232, s[28:31], s35 offen lds\n"
    "v_mfma_f32_16x16x32_bf16 v[68:71], a[140:143], v[36:39], v[68:71]\n"
    "s_add_u32 m0, s34, 45952\n"
    "v_cvt_pk_bf16_f32 v92, v160, v161\n"
    "v_cvt_pk_bf16_f32 v93, v162, v163\n"
    "v_cvt_pk_bf16_f32 v140, v172, v173\n"
    "v_cvt_pk_bf16_f32 v141, v174, v175\n"
    "v_cvt_pk_bf16_f32 v124, v164, v165\n"
    "v_cvt_pk_bf16_f32 v125, v166, v167\n"
    "s_waitcnt lgkmcnt(0)\n"
    "v_mfma_f32_16x16x32_bf16 v[152:155], v[140:143], v[92:95], v[152:155]\n"
    "ds_read_b128 v[172:175], v3 offset:5120\n"
    "v_cvt_pk_bf16_f32 v98, v160, v161\n"
    "v_mfma_f32_16x16x32_bf16 v[152:155], v[124:127], v[76:79], v[152:155]\n"
    "buffer_load_dwordx4 v232, s[28:31], s35 offen offset:128 lds\n"
    "s_add_u32 m0, s34, 46848\n"
    "v_cvt_pk_bf16_f32 v99, v162, v163\n"
    "v_cvt_pk_bf16_f32 v144, v196, v197\n"
    "v_cvt_pk_bf16_f32 v145, v198, v199\n"
    "v_mul_f32 v184, s75, v184\n"
    "v_mul_f32 v185, s75, v185\n"
    "v_mfma_f32_16x16x32_bf16 v[156:159], v[142:145], v[96:99], v[156:159]\n"
    "v_mul_f32 v186, s75, v186\n"
    "v_mul_f32 v187, s75, v187\n"
    "v_cvt_pk_bf16_f32 v100, v184, v185\n"
    "v_cvt_pk_bf16_f32 v101, v186, v187\n"
    "buffer_load_dwordx4 v232, s[28:31], s35 offen offset:256 lds\n"
    "s_add_u32 m0, s34, 57344\n"
    "s_mov_b32 s35, 185088\n"
    "v_cvt_pk_bf16_f32 v106, v184, v185\n"
    "v_cvt_pk_bf16_f32 v107, v186, v187\n"
    "v_cvt_pk_bf16_f32 v128, v188, v189\n"
    "v_cvt_pk_bf16_f32 v129, v190, v191\n"
    "v_cvt_pk_bf16_f32 v130, v192, v193\n"
    "v_cvt_pk_bf16_f32 v131, v194, v195\n"
    "ds_read_b128 v[184:187], v3 offset:8192\n"
    "v_mul_f32 v176, s75, v176\n"
    "v_mfma_f32_16x16x32_bf16 v[156:159], v[128:131], v[76:79], v[156:159]\n"
    "ds_read_b128 v[110:113], v0 offset:28672\n"
    "v_mul_f32 v177, s75, v177\n"
    "v_mfma_f32_16x16x32_bf16 v[168:171], v[140:143], v[100:103], v[168:171]\n"
    "v_mul_f32 v178, s75, v178\n"
    "v_mul_f32 v179, s75, v179\n"
    "v_mul_f32 v180, s75, v180\n"
    "v_mul_f32 v181, s75, v181\n"
    "v_mul_f32 v182, s75, v182\n"
    "v_mul_f32 v183, s75, v183\n"
    "v_cvt_pk_bf16_f32 v80, v176, v177\n"
    "v_cvt_pk_bf16_f32 v81, v178, v179\n"
    "v_cvt_pk_bf16_f32 v82, v180, v181\n"
    "v_cvt_pk_bf16_f32 v83, v182, v183\n"
    "ds_read_b128 v[188:191], v3 offset:9216\n"
    "v_mul_f32 v208, s75, v208\n"
    "v_mfma_f32_16x16x32_bf16 v[168:171], v[124:127], v[80:83], v[168:171]\n"
    "ds_read_b128 v[160:163], v3 offset:2048\n"
    "v_mul_f32 v209, s75, v209\n"
    "s_waitcnt lgkmcnt(3)\n"
    "v_mfma_f32_16x16x32_bf16 v[172:175], v[142:145], v[104:107], v[172:175]\n"
    "ds_read_b128 v[94:97], v1 offset:24576\n"
    "v_mul_f32 v210, s75, v210\n"
    "v_mfma_f32_16x16x32_bf16 v[172:175], v[128:131], v[80:83], v[172:175]\n"
    "v_mul_f32 v211, s75, v211\n"
    "v_cvt_pk_bf16_f32 v108, v208, v209\n"
    "v_cvt_pk_bf16_f32 v109, v210, v211\n"
    "v_mul_f32 v200, s75, v200\n"
    "v_mul_f32 v201, s75, v201\n"
    "v_mul_f32 v202, s75, v202\n"
    "s_waitcnt lgkmcnt(3)\n"
    "v_mfma_f32_16x16x32_bf16 v[184:187], v[140:143], v[108:111], v[184:187]\n"
    "v_mul_f32 v203, s75, v203\n"
    "v_mul_f32 v204, s75, v204\n"
    "v_mul_f32 v205, s75, v205\n"
    "v_mul_f32 v206, s75, v206\n"
    "v_mul_f32 v207, s75, v207\n"
    "v_cvt_pk_bf16_f32 v84, v200, v201\n"
    "v_cvt_pk_bf16_f32 v85, v202, v203\n"
    "v_cvt_pk_bf16_f32 v86, v204, v205\n"
    "v_cvt_pk_bf16_f32 v87, v206, v207\n"
    "ds_read_b128 v[164:167], v3 offset:3072\n"
    "v_cvt_pk_bf16_f32 v114, v208, v209\n"
    "v_mfma_f32_16x16x32_bf16 v[184:187], v[124:127], v[84:87], v[184:187]\n"
    "v_cvt_pk_bf16_f32 v115, v210, v211\n"
    "ds_read_b128 v[200:203], v3 offset:12288\n"
    "v_cvt_pk_bf16_f32 v146, v44, v45\n"
    "s_waitcnt lgkmcnt(2)\n"
    "v_mfma_f32_16x16x32_bf16 v[188:191], v[142:145], v[112:115], v[188:191]\n"
    "ds_read_b128 v[118:121], v0 offset:30720\n"
    "v_cvt_pk_bf16_f32 v147, v46, v47\n"
    "v_mfma_f32_16x16x32_bf16 v[188:191], v[128:131], v[84:87], v[188:191]\n"
    "v_cvt_pk_bf16_f32 v132, v212, v213\n"
    "v_cvt_pk_bf16_f32 v133, v214, v215\n"
    "v_mfma_f32_16x16x32_bf16 v[160:163], v[146:149], v[92:95], v[160:163]\n"
    "v_cvt_pk_bf16_f32 v134, v40, v41\n"
    "v_cvt_pk_bf16_f32 v135, v42, v43\n"
    "ds_read_b128 v[204:207], v3 offset:13312\n"
    "v_cvt_pk_bf16_f32 v150, v68, v69\n"
    "v_mfma_f32_16x16x32_bf16 v[160:163], v[132:135], v[76:79], v[160:163]\n"
    "v_cvt_pk_bf16_f32 v151, v70, v71\n"
    "v_cvt_pk_bf16_f32 v136, v60, v61\n"
    "v_cvt_pk_bf16_f32 v137, v62, v63\n"
    "v_cvt_pk_bf16_f32 v138, v64, v65\n"
    "v_cvt_pk_bf16_f32 v139, v66, v67\n"
    "ds_read_b128 v[176:179], v3 offset:6144\n"
    "v_mul_f32 v56, s75, v56\n"
    "ds_read_b128 v[102:105], v1 offset:26624\n"
    "v_mul_f32 v57, s75, v57\n"
    "s_waitcnt lgkmcnt(3)\n"
    "v_mfma_f32_16x16x32_bf16 v[164:167], v[148:151], v[96:99], v[164:167]\n"
    "v_mul_f32 v58, s75, v58\n"
    "v_mul_f32 v59, s75, v59\n"
    "v_mfma_f32_16x16x32_bf16 v[164:167], v[136:139], v[76:79], v[164:167]\n"
    "v_cvt_pk_bf16_f32 v116, v56, v57\n"
    "v_cvt_pk_bf16_f32 v117, v58, v59\n"
    "v_mul_f32 v48, s75, v48\n"
    "v_mul_f32 v49, s75, v49\n"
    "v_mfma_f32_16x16x32_bf16 v[200:203], v[140:143], v[116:119], v[200:203]\n"
    "v_mul_f32 v50, s75, v50\n"
    "v_mul_f32 v51, s75, v51\n"
    "v_mul_f32 v52, s75, v52\n"
    "v_mul_f32 v53, s75, v53\n"
    "v_mul_f32 v54, s75, v54\n"
    "v_mul_f32 v55, s75, v55\n"
    "v_cvt_pk_bf16_f32 v88, v48, v49\n"
    "v_cvt_pk_bf16_f32 v89, v50, v51\n"
    "v_cvt_pk_bf16_f32 v90, v52, v53\n"
    "v_cvt_pk_bf16_f32 v91, v54, v55\n"
    "ds_read_b128 v[180:183], v3 offset:7168\n"
    "v_cvt_pk_bf16_f32 v122, v56, v57\n"
    "v_mfma_f32_16x16x32_bf16 v[200:203], v[124:127], v[88:91], v[200:203]\n"
    "v_cvt_pk_bf16_f32 v123, v58, v59\n"
    "ds_read_b128 v[192:195], v3 offset:10240\n"
    "v_max3_f32 v140, v152, v153, v154\n"
    "s_waitcnt lgkmcnt(2)\n"
    "v_mfma_f32_16x16x32_bf16 v[204:207], v[142:145], v[120:123], v[204:207]\n"
    "ds_read_b128 v[110:113], v1 offset:28672\n"
    "v_max3_f32 v141, v168, v169, v170\n"
    "v_mfma_f32_16x16x32_bf16 v[204:207], v[128:131], v[88:91], v[204:207]\n"
    "ds_read_b128 v[196:199], v3 offset:11264\n"
    "v_max3_f32 v140, v140, v155, v156\n"
    "v_mfma_f32_16x16x32_bf16 v[176:179], v[146:149], v[100:103], v[176:179]\n"
    "ds_read_b128 v[208:211], v3 offset:14336\n"
    "v_max3_f32 v141, v141, v171, v172\n"
    "v_mfma_f32_16x16x32_bf16 v[176:179], v[132:135], v[80:83], v[176:179]\n"
    "ds_read_b128 v[118:121], v1 offset:30720\n"
    "v_max3_f32 v140, v140, v157, v158\n"
    "ds_read_b64_tr_b16 a[144:145], v216 offset:24576\n"
    "ds_read_b64_tr_b16 a[146:147], v216 offset:25088\n"
    "ds_read_b64_tr_b16 a[168:169], v217 offset:24576\n"
    "ds_read_b64_tr_b16 a[170:171], v217 offset:25088\n"
    "ds_read_b64_tr_b16 a[192:193], v218 offset:24576\n"
    "ds_read_b64_tr_b16 a[194:195], v218 offset:25088\n"
    "ds_read_b128 v[212:215], v3 offset:15360\n"
    "v_max3_f32 v141, v141, v173, v174\n"
    "ds_read_b128 v[24:27], v0 offset:12288\n"
    "s_waitcnt lgkmcnt(11)\n"
    "v_mfma_f32_16x16x32_bf16 v[180:183], v[148:151], v[104:107], v[180:183]\n"
    "ds_read_b128 v[28:31], v0 offset:18432\n"
    "v_max3_f32 v140, v140, v159, v160\n"
    "v_mfma_f32_16x16x32_bf16 v[180:183], v[136:139], v[80:83], v[180:183]\n"
    "ds_read_b128 v[32:35], v1 offset:0\n"
    "v_max3_f32 v141, v141, v175, v176\n"
    "ds_read_b128 v[36:39], v1 offset:6144\n"
    "v_mfma_f32_16x16x32_bf16 v[192:195], v[146:149], v[108:111], v[192:195]\n"
    "v_max3_f32 v140, v140, v161, v162\n"
    "v_max3_f32 v141, v141, v177, v178\n"
    "v_mfma_f32_16x16x32_bf16 v[192:195], v[132:135], v[84:87], v[192:195]\n"
    "v_max3_f32 v140, v140, v163, v164\n"
    "v_max3_f32 v141, v141, v179, v180\n"
    "s_waitcnt lgkmcnt(4)\n"
    "v_mfma_f32_16x16x32_bf16 v[196:199], v[148:151], v[112:115], v[196:199]\n"
    "v_max3_f32 v140, v140, v165, v166\n"
    "v_max3_f32 v141, v141, v181, v182\n"
    "v_mfma_f32_16x16x32_bf16 v[196:199], v[136:139], v[84:87], v[196:199]\n"
    "ds_read_b128 v[84:87], v1 offset:4096\n"
    "v_max_f32 v140, v140, v167\n"
    "v_mfma_f32_16x16x32_bf16 v[208:211], v[146:149], v[116:119], v[208:211]\n"
    "v_max_f32 v141, v141, v183\n"
    "v_max3_f32 v144, v184, v185, v186\n"
    "v_mfma_f32_16x16x32_bf16 v[208:211], v[132:135], v[88:91], v[208:211]\n"
    "s_waitcnt vmcnt(6)\n"
    "s_barrier\n"
    "v_mfma_f32_16x16x32_bf16 v[212:215], v[148:151], v[120:123], v[212:215]\n"
    "buffer_load_dwordx4 v232, s[28:31], s35 offen lds\n"
    "v_mfma_f32_16x16x32_bf16 v[212:215], v[136:139], v[88:91], v[212:215]\n"
    "s_add_u32 m0, s34, 58240\n"
    "ds_read_b64_tr_b16 a[148:149], v216 offset:36864\n"
    "v_mfma_f32_16x16x32_bf16 v[40:43], v[16:19], a[144:147], 0\n"
    "ds_read_b64_tr_b16 a[150:151], v216 offset:37376\n"
    "ds_read_b64_tr_b16 a[172:173], v217 offset:36864\n"
    "v_mfma_f32_16x16x32_bf16 v[44:47], v[16:19], a[168:171], 0\n"
    "ds_read_b64_tr_b16 a[174:175], v217 offset:37376\n"
    "ds_read_b64_tr_b16 a[196:197], v218 offset:36864\n"
    "v_mfma_f32_16x16x32_bf16 v[48:51], v[16:19], a[192:195], 0\n"
    "buffer_load_dwordx4 v232, s[28:31], s35 offen offset:128 lds\n"
    "v_mfma_f32_16x16x32_bf16 v[52:55], v[20:23], a[144:147], 0\n"
    "s_add_u32 m0, s34, 59136\n"
    "ds_read_b64_tr_b16 a[198:199], v218 offset:37376\n"
    "v_mfma_f32_16x16x32_bf16 v[56:59], v[20:23], a[168:171], 0\n"
    "ds_read_b64_tr_b16 a[152:153], v216 offset:49152\n"
    "ds_read_b64_tr_b16 a[154:155], v216 offset:49664\n"
    "v_mfma_f32_16x16x32_bf16 v[60:63], v[20:23], a[192:195], 0\n"
    "ds_read_b64_tr_b16 a[176:177], v217 offset:49152\n"
    "ds_read_b64_tr_b16 a[178:179], v217 offset:49664\n"
    "s_waitcnt lgkmcnt(6)\n"
    "v_mfma_f32_16x16x32_bf16 v[64:67], v[24:27], a[144:147], 0\n"
    "buffer_load_dwordx4 v232, s[28:31], s35 offen offset:256 lds\n"
    "v_mfma_f32_16x16x32_bf16 v[68:71], v[24:27], a[168:171], 0\n"
    "s_add_u32 m0, s34, 69632\n"
    "s_mov_b32 s35, 0\n"
    "v_mfma_f32_16x16x32_bf16 v[72:75], v[24:27], a[192:195], 0\n"
    "ds_read_b64_tr_b16 a[200:201], v218 offset:49152\n"
    "ds_read_b64_tr_b16 a[202:203], v218 offset:49664\n"
    "v_mfma_f32_16x16x32_bf16 v[100:103], v[28:31], a[144:147], 0\n"
    "ds_read_b128 v[16:19], v1 offset:12288\n"
    "v_permlane32_swap_b32 v140, v141\n"
    "v_mfma_f32_16x16x32_bf16 v[104:107], v[28:31], a[168:171], 0\n"
    "ds_read_b128 v[20:23], v1 offset:18432\n"
    "v_max_f32 v140, v140, v141\n"
    "v_mfma_f32_16x16x32_bf16 v[108:111], v[28:31], a[192:195], 0\n"
    "ds_read_b128 v[24:27], v0 offset:2048\n"
    "v_mov_b32 v141, v140\n"
    "v_mfma_f32_16x16x32_bf16 v[40:43], v[32:35], a[148:151], v[40:43]\n"
    "ds_read_b128 v[28:31], v0 offset:8192\n"
    "v_permlane16_swap_b32 v140, v141\n"
    "v_mfma_f32_16x16x32_bf16 v[44:47], v[32:35], a[172:175], v[44:47]\n"
    "ds_read_b128 v[88:91], v1 offset:10240\n"
    "v_max_f32 v140, v140, v141\n"
    "s_waitcnt lgkmcnt(5)\n"
    "v_mfma_f32_16x16x32_bf16 v[48:51], v[32:35], a[196:199], v[48:51]\n"
    "ds_read_b128 v[32:35], v0 offset:14336\n"
    "v_mul_f32 v140, s74, v140\n"
    "v_mfma_f32_16x16x32_bf16 v[52:55], v[36:39], a[148:151], v[52:55]\n"
    "ds_read_b128 v[132:135], v1 offset:16384\n"
    "v_mov_b32 v141, v140\n"
    "v_mfma_f32_16x16x32_bf16 v[56:59], v[36:39], a[172:175], v[56:59]\n"
    "ds_read_b128 v[136:139], v1 offset:22528\n"
    "v_permlane32_swap_b32 v140, v141\n"
    "v_mfma_f32_16x16x32_bf16 v[60:63], v[36:39], a[196:199], v[60:63]\n"
    "ds_read_b128 v[36:39], v0 offset:20480\n"
    "v_fma_f32 v152, v152, s73, v140\n"
    "s_waitcnt lgkmcnt(5)\n"
    "v_mfma_f32_16x16x32_bf16 v[64:67], v[16:19], a[148:151], v[64:67]\n"
    "s_waitcnt vmcnt(3)\n"
    "s_barrier\n"
    "v_mfma_f32_16x16x32_bf16 v[68:71], v[16:19], a[172:175], v[68:71]\n"
    "buffer_load_dwordx4 v241, s[84:87], s35 offen lds\n"
    "v_mfma_f32_16x16x32_bf16 v[72:75], v[16:19], a[196:199], v[72:75]\n"
    "s_add_u32 m0, s34, 70528\n"
    "ds_read_b64_tr_b16 a[156:157], v216 offset:0\n"
    "v_mfma_f32_16x16x32_bf16 v[100:103], v[20:23], a[148:151], v[100:103]\n"
    "ds_read_b64_tr_b16 a[158:159], v216 offset:512\n"
    "ds_read_b128 v[16:19], v1 offset:2048\n"
    "v_mfma_f32_16x16x32_bf16 v[104:107], v[20:23], a[172:175], v[104:107]\n"
    "ds_read_b64_tr_b16 a[180:181], v217 offset:0\n"
    "ds_read_b64_tr_b16 a[182:183], v217 offset:512\n"
    "v_mfma_f32_16x16x32_bf16 v[108:111], v[20:23], a[196:199], v[108:111]\n"
    "buffer_load_dwordx4 v241, s[84:87], s35 offen offset:128 lds\n"
    "v_mfma_f32_16x16x32_bf16 v[40:43], v[24:27], a[152:155], v[40:43]\n"
    "s_add_u32 m0, s34, 71424\n"
    "ds_read_b64_tr_b16 a[204:205], v218 offset:0\n"
    "v_mfma_f32_16x16x32_bf16 v[44:47], v[24:27], a[176:179], v[44:47]\n"
    "ds_read_b64_tr_b16 a[206:207], v218 offset:512\n"
    "ds_read_b128 v[20:23], v1 offset:8192\n"
    "v_mfma_f32_16x16x32_bf16 v[48:51], v[24:27], a[200:203], v[48:51]\n"
    "ds_read_b64_tr_b16 a[160:161], v216 offset:12288\n"
    "ds_read_b64_tr_b16 a[162:163], v216 offset:12800\n"
    "v_mfma_f32_16x16x32_bf16 v[52:55], v[28:31], a[152:155], v[52:55]\n"
    "buffer_load_dwordx4 v241, s[84:87], s35 offen offset:256 lds\n"
    "v_mfma_f32_16x16x32_bf16 v[56:59], v[28:31], a[176:179], v[56:59]\n"
    "s_add_u32 m0, s34, 81920\n"
    "s_mov_b32 s35, 18432\n"
    "v_mfma_f32_16x16x32_bf16 v[60:63], v[28:31], a[200:203], v[60:63]\n"
    "ds_read_b64_tr_b16 a[184:185], v217 offset:12288\n"
    "ds_read_b64_tr_b16 a[186:187], v217 offset:12800\n"
    "s_waitcnt lgkmcnt(5)\n"
    "v_mfma_f32_16x16x32_bf16 v[64:67], v[32:35], a[152:155], v[64:67]\n"
    "ds_read_b64_tr_b16 a[208:209], v218 offset:12288\n"
    "ds_read_b64_tr_b16 a[210:211], v218 offset:12800\n"
    "v_mfma_f32_16x16x32_bf16 v[68:71], v[32:35], a[176:179], v[68:71]\n"
    "buffer_load_dwordx4 v241, s[84:87], s35 offen lds\n"
    "v_mfma_f32_16x16x32_bf16 v[72:75], v[32:35], a[200:203], v[72:75]\n"
    "s_add_u32 m0, s34, 82816\n"
    "ds_read_b128 v[24:27], v1 offset:14336\n"
    "v_mfma_f32_16x16x32_bf16 v[100:103], v[36:39], a[152:155], v[100:103]\n"
    "ds_read_b128 v[28:31], v1 offset:20480\n"
    "v_fma_f32 v153, v153, s73, v140\n"
    "v_mfma_f32_16x16x32_bf16 v[104:107], v[36:39], a[176:179], v[104:107]\n"
    "ds_read_b128 v[32:35], v0 offset:4096\n"
    "v_fma_f32 v154, v154, s73, v140\n"
    "v_mfma_f32_16x16x32_bf16 v[108:111], v[36:39], a[200:203], v[108:111]\n"
    "buffer_load_dwordx4 v241, s[84:87], s35 offen offset:128 lds\n"
    "v_mfma_f32_16x16x32_bf16 v[40:43], v[16:19], a[156:159], v[40:43]\n"
    "s_add_u32 m0, s34, 83712\n"
    "ds_read_b128 v[36:39], v0 offset:10240\n"
    "v_mfma_f32_16x16x32_bf16 v[44:47], v[16:19], a[180:183], v[44:47]\n"
    "v_exp_f32 v152, v152\n"
    "v_mfma_f32_16x16x32_bf16 v[48:51], v[16:19], a[204:207], v[48:51]\n"
    "ds_read_b128 v[16:19], v0 offset:16384\n"
    "v_fma_f32 v155, v155, s73, v140\n"
    "s_waitcnt lgkmcnt(2)\n"
    "v_mfma_f32_16x16x32_bf16 v[52:55], v[20:23], a[156:159], v[52:55]\n"
    "buffer_load_dwordx4 v241, s[84:87], s35 offen offset:256 lds\n"
    "v_mfma_f32_16x16x32_bf16 v[56:59], v[20:23], a[180:183], v[56:59]\n"
    "s_add_u32 m0, s34, 0\n"
    "s_mov_b32 s35, 36864\n"
    "v_mfma_f32_16x16x32_bf16 v[60:63], v[20:23], a[204:207], v[60:63]\n"
    "ds_read_b128 v[20:23], v0 offset:22528\n"
    "v_fma_f32 v156, v156, s73, v140\n"
    "v_mfma_f32_16x16x32_bf16 v[64:67], v[24:27], a[156:159], v[64:67]\n"
    "v_exp_f32 v153, v153\n"
    "v_mfma_f32_16x16x32_bf16 v[68:71], v[24:27], a[180:183], v[68:71]\n"
    "v_exp_f32 v154, v154\n"
    "v_mfma_f32_16x16x32_bf16 v[72:75], v[24:27], a[204:207], v[72:75]\n"
    "v_exp_f32 v155, v155\n"
    "v_mfma_f32_16x16x32_bf16 v[100:103], v[28:31], a[156:159], v[100:103]\n"
    "v_fma_f32 v157, v157, s73, v140\n"
    "v_fma_f32 v158, v158, s73, v140\n"
    "v_mfma_f32_16x16x32_bf16 v[104:107], v[28:31], a[180:183], v[104:107]\n"
    "v_fma_f32 v159, v159, s73, v140\n"
    "v_cvt_pk_bf16_f32 v152, v152, v153\n"
    "v_mfma_f32_16x16x32_bf16 v[108:111], v[28:31], a[204:207], v[108:111]\n"
    "v_exp_f32 v156, v156\n"
    "v_mfma_f32_16x16x32_bf16 v[40:43], v[32:35], a[160:163], v[40:43]\n"
    "v_exp_f32 v157, v157\n"
    "v_mfma_f32_16x16x32_bf16 v[44:47], v[32:35], a[184:187], v[44:47]\n"
    "v_exp_f32 v158, v158\n"
    "v_mfma_f32_16x16x32_bf16 v[48:51], v[32:35], a[208:211], v[48:51]\n"
    "v_exp_f32 v159, v159\n"
    "s_waitcnt lgkmcnt(0)\n"
    "v_mfma_f32_16x16x32_bf16 v[52:55], v[36:39], a[160:163], v[52:55]\n"
    "v_cvt_pk_bf16_f32 v153, v154, v155\n"
    "v_cvt_pk_bf16_f32 v154, v156, v157\n"
    "v_mfma_f32_16x16x32_bf16 v[56:59], v[36:39], a[184:187], v[56:59]\n"
    "v_cvt_pk_bf16_f32 v155, v158, v159\n"
    "v_fma_f32 v160, v160, s73, v140\n"
    "v_mfma_f32_16x16x32_bf16 v[60:63], v[36:39], a[208:211], v[60:63]\n"
    "v_fma_f32 v161, v161, s73, v140\n"
    "v_fma_f32 v162, v162, s73, v140\n"
    "v_mfma_f32_16x16x32_bf16 v[64:67], v[16:19], a[160:163], v[64:67]\n"
    "v_fma_f32 v163, v163, s73, v140\n"
    "v_fma_f32 v164, v164, s73, v140\n"
    "v_mfma_f32_16x16x32_bf16 v[68:71], v[16:19], a[184:187], v[68:71]\n"
    "v_fma_f32 v165, v165, s73, v140\n"
    "v_fma_f32 v166, v166, s73, v140\n"
    "v_mfma_f32_16x16x32_bf16 v[72:75], v[16:19], a[208:211], v[72:75]\n"
    "v_fma_f32 v167, v167, s73, v140\n"
    "v_fma_f32 v168, v168, s73, v141\n"
    "v_mfma_f32_16x16x32_bf16 v[100:103], v[20:23], a[160:163], v[100:103]\n"
    "v_exp_f32 v160, v160\n"
    "v_mfma_f32_16x16x32_bf16 v[104:107], v[20:23], a[184:187], v[104:107]\n"
    "v_exp_f32 v161, v161\n"
    "v_mfma_f32_16x16x32_bf16 v[108:111], v[20:23], a[208:211], v[108:111]\n"
    "v_exp_f32 v162, v162\n"
    "v_exp_f32 v163, v163\n"
    "v_exp_f32 v164, v164\n"
    "v_exp_f32 v165, v165\n"
    "v_exp_f32 v166, v166\n"
    "v_exp_f32 v167, v167\n"
    "v_cvt_pk_bf16_f32 v156, v160, v161\n"
    "v_cvt_pk_bf16_f32 v157, v162, v163\n"
    "v_cvt_pk_bf16_f32 v158, v164, v165\n"
    "v_cvt_pk_bf16_f32 v159, v166, v167\n"
    "v_fma_f32 v169, v169, s73, v141\n"
    "v_exp_f32 v168, v168\n"
    "v_exp_f32 v169, v169\n"
    "v_fma_f32 v170, v170, s73, v141\n"
    "v_fma_f32 v171, v171, s73, v141\n"
    "v_exp_f32 v170, v170\n"
    "v_exp_f32 v171, v171\n"
    "v_fma_f32 v172, v172, s73, v141\n"
    "v_fma_f32 v173, v173, s73, v141\n"
    "v_fma_f32 v174, v174, s73, v141\n"
    "v_fma_f32 v175, v175, s73, v141\n"
    "v_cvt_pk_bf16_f32 v168, v168, v169\n"
    "v_exp_f32 v172, v172\n"
    "v_exp_f32 v173, v173\n"
    "s_waitcnt vmcnt(6)\n"
    "s_barrier\n"
    "buffer_load_dwordx4 v241, s[84:87], s35 offen lds\n"
    "s_add_u32 m0, s34, 896\n"
    "ds_read_b64_tr_b16 a[164:165], v216 offset:24576\n"
    "ds_read_b64_tr_b16 a[166:167], v216 offset:25088\n"
    "ds_read_b64_tr_b16 a[188:189], v217 offset:24576\n"
    "ds_read_b64_tr_b16 a[190:191], v217 offset:25088\n"
    "ds_read_b64_tr_b16 a[212:213], v218 offset:24576\n"
    "ds_read_b64_tr_b16 a[214:215], v218 offset:25088\n"
    "v_exp_f32 v174, v174\n"
    "v_exp_f32 v175, v175\n"
    "v_cvt_pk_bf16_f32 v169, v170, v171\n"
    "buffer_load_dwordx4 v241, s[84:87], s35 offen offset:128 lds\n"
    "s_add_u32 m0, s34, 1792\n"
    "v_fma_f32 v180, v180, s73, v141\n"
    "v_fma_f32 v181, v181, s73, v141\n"
    "v_fma_f32 v182, v182, s73, v141\n"
    "v_fma_f32 v183, v183, s73, v141\n"
    "v_cvt_pk_bf16_f32 v170, v172, v173\n"
    "v_cvt_pk_bf16_f32 v171, v174, v175\n"
    "v_exp_f32 v180, v180\n"
    "v_exp_f32 v181, v181\n"
    "v_exp_f32 v182, v182\n"
    "buffer_load_dwordx4 v241, s[84:87], s35 offen offset:256 lds\n"
    "s_waitcnt lgkmcnt(0)\n"
    "v_mfma_f32_16x16x32_bf16 v[40:43], v[84:87], a[164:167], v[40:43]\n"
    "s_add_u32 m0, s34, 12288\n"
    "s_mov_b32 s35, 55296\n"
    "v_mfma_f32_16x16x32_bf16 v[44:47], v[84:87], a[188:191], v[44:47]\n"
    "v_exp_f32 v183, v183\n"
    "v_mfma_f32_16x16x32_bf16 v[48:51], v[84:87], a[212:215], v[48:51]\n"
    "v_cvt_pk_bf16_f32 v174, v180, v181\n"
    "v_cvt_pk_bf16_f32 v175, v182, v183\n"
    "v_mfma_f32_16x16x32_bf16 v[52:55], v[88:91], a[164:167], v[52:55]\n"
    "buffer_load_dwordx4 v241, s[84:87], s35 offen lds\n"
    "v_mfma_f32_16x16x32_bf16 v[56:59], v[88:91], a[188:191], v[56:59]\n"
    "s_add_u32 m0, s34, 13184\n"
    "v_cvt_pk_bf16_f32 v76, v40, v41\n"
    "v_mfma_f32_16x16x32_bf16 v[60:63], v[88:91], a[212:215], v[60:63]\n"
    "v_cvt_pk_bf16_f32 v77, v42, v43\n"
    "v_cvt_pk_bf16_f32 v124, v44, v45\n"
    "v_mfma_f32_16x16x32_bf16 v[64:67], v[132:135], a[164:167], v[64:67]\n"
    "v_cvt_pk_bf16_f32 v78, v52, v53\n"
    "v_cvt_pk_bf16_f32 v79, v54, v55\n"
    "v_mfma_f32_16x16x32_bf16 v[68:71], v[132:135], a[188:191], v[68:71]\n"
    "buffer_load_dwordx4 v241, s[84:87], s35 offen offset:128 lds\n"
    "v_mfma_f32_16x16x32_bf16 v[72:75], v[132:135], a[212:215], v[72:75]\n"
    "s_add_u32 m0, s34, 14080\n"
    "v_cvt_pk_bf16_f32 v125, v46, v47\n"
    "v_mfma_f32_16x16x32_bf16 v[100:103], v[136:139], a[164:167], v[100:103]\n"
    "v_cvt_pk_bf16_f32 v80, v64, v65\n"
    "v_cvt_pk_bf16_f32 v81, v66, v67\n"
    "v_mfma_f32_16x16x32_bf16 v[104:107], v[136:139], a[188:191], v[104:107]\n"
    "v_cvt_pk_bf16_f32 v126, v56, v57\n"
    "v_cvt_pk_bf16_f32 v127, v58, v59\n"
    "v_mfma_f32_16x16x32_bf16 v[108:111], v[136:139], a[212:215], v[108:111]\n"
    "buffer_load_dwordx4 v241, s[84:87], s35 offen offset:256 lds\n"
    "v_mfma_f32_16x16x32_bf16 v[52:55], v[12:15], v[152:155], 0\n"
    "s_add_u32 m0, s33, 32768\n"
    "v_cvt_pk_bf16_f32 v82, v100, v101\n"
    "v_mfma_f32_16x16x32_bf16 v[52:55], v[12:15], v[156:159], v[52:55]\n"
    "v_cvt_pk_bf16_f32 v83, v102, v103\n"
    "v_cvt_pk_bf16_f32 v128, v68, v69\n"
    "v_mfma_f32_16x16x32_bf16 v[40:43], v[76:79], v[152:155], 0\n"
    "v_cvt_pk_bf16_f32 v129, v70, v71\n"
    "v_cvt_pk_bf16_f32 v130, v104, v105\n"
    "v_mfma_f32_16x16x32_bf16 v[40:43], v[80:83], v[156:159], v[40:43]\n"
    "buffer_load_dwordx4 v6, s[16:19], s65 offen nt lds\n"
    "v_mfma_f32_16x16x32_bf16 v[44:47], v[124:127], v[152:155], 0\n"
    "s_add_u32 m0, s33, 33792\n"
    "v_cvt_pk_bf16_f32 v131, v106, v107\n"
    "v_cvt_pk_bf16_f32 v92, v48, v49\n"
    "v_cvt_pk_bf16_f32 v93, v50, v51\n"
    "v_mfma_f32_16x16x32_bf16 v[44:47], v[128:131], v[156:159], v[44:47]\n"
    "v_cvt_pk_bf16_f32 v94, v60, v61\n"
    "v_cvt_pk_bf16_f32 v95, v62, v63\n"
    "v_cvt_pk_bf16_f32 v96, v72, v73\n"
    "v_cvt_pk_bf16_f32 v97, v74, v75\n"
    "v_mfma_f32_16x16x32_bf16 v[48:51], v[92:95], v[152:155], 0\n"
    "buffer_load_dwordx4 v7, s[16:19], s65 offen nt lds\n"
    "s_add_u32 m0, s33, 34688\n"
    "v_cvt_pk_bf16_f32 v98, v108, v109\n"
    "v_cvt_pk_bf16_f32 v99, v110, v111\n"
    "v_fma_f32 v176, v176, s73, v141\n"
    "v_fma_f32 v177, v177, s73, v141\n"
    "v_mfma_f32_16x16x32_bf16 v[48:51], v[96:99], v[156:159], v[48:51]\n"
    "v_fma_f32 v178, v178, s73, v141\n"
    "v_fma_f32 v179, v179, s73, v141\n"
    "v_mfma_f32_16x16x32_bf16 v[180:183], v[12:15], v[168:171], 0\n"
    "v_exp_f32 v176, v176\n"
    "buffer_load_dwordx4 v6, s[16:19], s65 offen offset:128 nt lds\n"
    "s_add_u32 m0, s33, 35712\n"
    "s_waitcnt vmcnt(9)\n"
    "s_barrier\n"
    "v_exp_f32 v177, v177\n"
    "v_exp_f32 v178, v178\n"
    "buffer_load_dwordx4 v7, s[16:19], s65 offen offset:128 nt lds\n"
    "s_add_u32 m0, s33, 36608\n"
    "v_exp_f32 v179, v179\n"
    "v_cvt_pk_bf16_f32 v172, v176, v177\n"
    "v_cvt_pk_bf16_f32 v173, v178, v179\n"
    "v_max3_f32 v145, v200, v201, v202\n"
    "v_max3_f32 v144, v144, v187, v188\n"
    "v_mfma_f32_16x16x32_bf16 v[180:183], v[12:15], v[172:175], v[180:183]\n"
    "v_max3_f32 v145, v145, v203, v204\n"
    "v_max3_f32 v144, v144, v189, v190\n"
    "v_mfma_f32_16x16x32_bf16 v[160:163], v[76:79], v[168:171], 0\n"
    "buffer_load_dwordx4 v6, s[16:19], s65 offen offset:256 nt lds\n"
    "v_mfma_f32_16x16x32_bf16 v[160:163], v[80:83], v[172:175], v[160:163]\n"
    "s_add_u32 m0, s33, 37632\n"
    "v_max3_f32 v145, v145, v205, v206\n"
    "v_mfma_f32_16x16x32_bf16 v[164:167], v[124:127], v[168:171], 0\n"
    "ds_read_b64_tr_b16 a[216:217], v220 offset:20480\n"
    "ds_read_b64_tr_b16 a[240:241], v221 offset:20480\n"
    "v_mfma_f32_16x16x32_bf16 v[164:167], v[128:131], v[172:175], v[164:167]\n"
    "ds_read_b64_tr_b16 v[232:233], v223 offset:20480\n"
    "ds_read_b64_tr_b16 a[218:219], v220 offset:26624\n"
    "v_mfma_f32_16x16x32_bf16 v[176:179], v[92:95], v[168:171], 0\n"
    "buffer_load_dwordx4 v7, s[16:19], s65 offen offset:256 nt lds\n"
    "v_mfma_f32_16x16x32_bf16 v[176:179], v[96:99], v[172:175], v[176:179]\n"
    "s_add_u32 m0, s79, 32768\n"
    "ds_read_b64_tr_b16 a[242:243], v221 offset:26624\n"
    "ds_read_b64_tr_b16 v[234:235], v223 offset:26624\n"
    "ds_read_b64_tr_b16 a[220:221], v220 offset:32768\n"
    "ds_read_b64_tr_b16 a[244:245], v221 offset:32768\n"
    "ds_read_b64_tr_b16 v[236:237], v223 offset:32768\n"
    "ds_read_b64_tr_b16 a[222:223], v220 offset:38912\n"
    "ds_read_b64_tr_b16 a[246:247], v221 offset:38912\n"
    "ds_read_b64_tr_b16 v[238:239], v223 offset:38912\n"
    "v_max3_f32 v144, v144, v191, v192\n"
    "buffer_load_dwordx4 v8, s[24:27], s72 offen nt lds\n"
    "s_add_u32 m0, s79, 33792\n"
    "v_max3_f32 v145, v145, v207, v208\n"
    "v_max3_f32 v144, v144, v193, v194\n"
    "v_max3_f32 v145, v145, v209, v210\n"
    "v_max3_f32 v144, v144, v195, v196\n"
    "v_max3_f32 v145, v145, v211, v212\n"
    "v_max3_f32 v144, v144, v197, v198\n"
    "v_max3_f32 v145, v145, v213, v214\n"
    "v_max_f32 v144, v144, v199\n"
    "v_max_f32 v145, v145, v215\n"
    "v_add_u32 v146, s34, v2\n"
    "v_add_u32 v220, 4294918144, v220\n"
    "buffer_load_dwordx4 v9, s[24:27], s72 offen nt lds\n"
    "v_permlane32_swap_b32 v144, v145\n"
    "v_max_f32 v144, v144, v145\n"
    "v_mov_b32 v145, v144\n"
    "v_add_u32 v221, 4294918144, v221\n"
    "v_add_u32 v223, 4294918144, v223\n"
    "v_permlane16_swap_b32 v144, v145\n"
    "v_max_f32 v144, v144, v145\n"
    "v_mul_f32 v144, s74, v144\n"
    "v_mov_b32 v145, v144\n"
    "v_xor_b32 v0, 32768, v0\n"
    "v_xor_b32 v1, 32768, v1\n"
    "v_permlane32_swap_b32 v144, v145\n"
    "v_fma_f32 v184, v184, s73, v144\n"
    "v_fma_f32 v185, v185, s73, v144\n"
    "v_exp_f32 v184, v184\n"
    "v_exp_f32 v185, v185\n"
    "v_fma_f32 v186, v186, s73, v144\n"
    "v_fma_f32 v187, v187, s73, v144\n"
    "v_exp_f32 v186, v186\n"
    "v_exp_f32 v187, v187\n"
    "v_fma_f32 v188, v188, s73, v144\n"
    "v_fma_f32 v189, v189, s73, v144\n"
    "v_fma_f32 v190, v190, s73, v144\n"
    "v_fma_f32 v191, v191, s73, v144\n"
    "v_cvt_pk_bf16_f32 v184, v184, v185\n"
    "v_exp_f32 v188, v188\n"
    "v_exp_f32 v189, v189\n"
    "v_exp_f32 v190, v190\n"
    "v_exp_f32 v191, v191\n"
    "v_cvt_pk_bf16_f32 v185, v186, v187\n"
    "v_cvt_pk_bf16_f32 v186, v188, v189\n"
    "v_cvt_pk_bf16_f32 v187, v190, v191\n"
    "v_fma_f32 v192, v192, s73, v144\n"
    "v_fma_f32 v193, v193, s73, v144\n"
    "v_mfma_f32_16x16x32_bf16 v[136:139], v[12:15], v[184:187], 0\n"
    "v_fma_f32 v194, v194, s73, v144\n"
    "v_fma_f32 v195, v195, s73, v144\n"
    "v_fma_f32 v196, v196, s73, v144\n"
    "v_fma_f32 v197, v197, s73, v144\n"
    "v_fma_f32 v198, v198, s73, v144\n"
    "v_fma_f32 v199, v199, s73, v144\n"
    "v_exp_f32 v192, v192\n"
    "v_exp_f32 v193, v193\n"
    "v_exp_f32 v194, v194\n"
    "v_exp_f32 v195, v195\n"
    "v_exp_f32 v196, v196\n"
    "v_exp_f32 v197, v197\n"
    "v_exp_f32 v198, v198\n"
    "v_exp_f32 v199, v199\n"
    "v_cvt_pk_bf16_f32 v188, v192, v193\n"
    "v_cvt_pk_bf16_f32 v189, v194, v195\n"
    "v_cvt_pk_bf16_f32 v190, v196, v197\n"
    "v_cvt_pk_bf16_f32 v191, v198, v199\n"
    "v_fma_f32 v200, v200, s73, v145\n"
    "v_fma_f32 v201, v201, s73, v145\n"
    "v_mfma_f32_16x16x32_bf16 v[136:139], v[12:15], v[188:191], v[136:139]\n"
    "v_exp_f32 v200, v200\n"
    "v_mfma_f32_16x16x32_bf16 v[84:87], v[76:79], v[184:187], 0\n"
    "v_exp_f32 v201, v201\n"
    "v_mfma_f32_16x16x32_bf16 v[84:87], v[80:83], v[188:191], v[84:87]\n"
    "v_fma_f32 v202, v202, s73, v145\n"
    "v_fma_f32 v203, v203, s73, v145\n"
    "v_mfma_f32_16x16x32_bf16 v[88:91], v[124:127], v[184:187], 0\n"
    "v_exp_f32 v202, v202\n"
    "v_mfma_f32_16x16x32_bf16 v[88:91], v[128:131], v[188:191], v[88:91]\n"
    "v_exp_f32 v203, v203\n"
    "v_mfma_f32_16x16x32_bf16 v[132:135], v[92:95], v[184:187], 0\n"
    "v_fma_f32 v204, v204, s73, v145\n"
    "v_fma_f32 v205, v205, s73, v145\n"
    "v_mfma_f32_16x16x32_bf16 v[132:135], v[96:99], v[188:191], v[132:135]\n"
    "v_fma_f32 v206, v206, s73, v145\n"
    "v_fma_f32 v207, v207, s73, v145\n"
    "v_cvt_pk_bf16_f32 v200, v200, v201\n"
    "v_exp_f32 v204, v204\n"
    "v_exp_f32 v205, v205\n"
    "v_exp_f32 v206, v206\n"
    "v_exp_f32 v207, v207\n"
    "v_cvt_pk_bf16_f32 v201, v202, v203\n"
    "v_fma_f32 v212, v212, s73, v145\n"
    "v_fma_f32 v213, v213, s73, v145\n"
    "v_fma_f32 v214, v214, s73, v145\n"
    "v_fma_f32 v215, v215, s73, v145\n"
    "v_cvt_pk_bf16_f32 v202, v204, v205\n"
    "v_cvt_pk_bf16_f32 v203, v206, v207\n"
    "v_exp_f32 v212, v212\n"
    "v_exp_f32 v213, v213\n"
    "v_exp_f32 v214, v214\n"
    "v_exp_f32 v215, v215\n"
    "v_cvt_pk_bf16_f32 v206, v212, v213\n"
    "s_waitcnt vmcnt(8) lgkmcnt(0)\n"
    "s_barrier\n"
    "ds_write_b128 v146, a[232:235] offset:4096\n"
    "ds_read_b64_tr_b16 a[224:225], v220 offset:0\n"
    "ds_read_b64_tr_b16 a[248:249], v221 offset:0\n"
    "ds_read_b64_tr_b16 v[240:241], v223 offset:0\n"
    "ds_read_b64_tr_b16 a[226:227], v220 offset:6144\n"
    "ds_read_b64_tr_b16 a[250:251], v221 offset:6144\n"
    "ds_read_b64_tr_b16 v[242:243], v223 offset:6144\n"
    "ds_write_b128 v146, v[224:227] offset:5120\n"
    "ds_read_b64_tr_b16 a[228:229], v220 offset:12288\n"
    "ds_read_b64_tr_b16 a[252:253], v221 offset:12288\n"
    "ds_read_b64_tr_b16 v[244:245], v223 offset:12288\n"
    "ds_read_b64_tr_b16 a[230:231], v220 offset:18432\n"
    "ds_read_b64_tr_b16 a[254:255], v221 offset:18432\n"
    "ds_read_b64_tr_b16 v[246:247], v223 offset:18432\n"
    "ds_write_b128 v146, v[248:251] offset:6144\n"
    "v_cvt_pk_bf16_f32 v207, v214, v215\n"
    "v_mfma_f32_16x16x32_bf16 v[212:215], v[12:15], v[200:203], 0\n"
    "v_fma_f32 v208, v208, s73, v145\n"
    "v_fma_f32 v209, v209, s73, v145\n"
    "v_fma_f32 v210, v210, s73, v145\n"
    "ds_write_b128 v146, a[236:239] offset:16384\n"
    "v_fma_f32 v211, v211, s73, v145\n"
    "v_exp_f32 v208, v208\n"
    "v_exp_f32 v209, v209\n"
    "v_exp_f32 v210, v210\n"
    "ds_write_b128 v146, v[228:231] offset:17408\n"
    "v_exp_f32 v211, v211\n"
    "v_cvt_pk_bf16_f32 v204, v208, v209\n"
    "v_cvt_pk_bf16_f32 v205, v210, v211\n"
    "v_add_u32 v220, 49152, v220\n"
    "v_add_u32 v221, 49152, v221\n"
    "v_mfma_f32_16x16x32_bf16 v[212:215], v[12:15], v[204:207], v[212:215]\n"
    "ds_write_b128 v146, v[252:255] offset:18432\n"
    "v_mfma_f32_16x16x32_bf16 v[192:195], v[76:79], v[200:203], 0\n"
    "v_add_u32 v223, 49152, v223\n"
    "s_mov_b32 s65, s66\n"
    "v_mfma_f32_16x16x32_bf16 v[192:195], v[80:83], v[204:207], v[192:195]\n"
    "s_mov_b32 s72, s0\n"
    "s_mov_b32 s66, s67\n"
    "v_mfma_f32_16x16x32_bf16 v[196:199], v[124:127], v[200:203], 0\n"
    "v_mfma_f32_16x16x32_bf16 v[196:199], v[128:131], v[204:207], v[196:199]\n"
    "v_mfma_f32_16x16x32_bf16 v[208:211], v[92:95], v[200:203], 0\n"
    "v_mfma_f32_16x16x32_bf16 v[208:211], v[96:99], v[204:207], v[208:211]\n"
    "s_waitcnt vmcnt(0) lgkmcnt(0)\n"
    "s_barrier\n"
    "ds_read_b64_tr_b16 a[232:233], v220 offset:20480\n"
    "ds_read_b64_tr_b16 v[224:225], v221 offset:20480\n"
    "ds_read_b64_tr_b16 v[248:249], v223 offset:20480\n"
    "ds_read_b64_tr_b16 a[234:235], v220 offset:26624\n"
    "ds_read_b64_tr_b16 v[226:227], v221 offset:26624\n"
    "ds_read_b64_tr_b16 v[250:251], v223 offset:26624\n"
    "ds_read_b64_tr_b16 a[236:237], v220 offset:32768\n"
    "ds_read_b64_tr_b16 v[228:229], v221 offset:32768\n"
    "ds_read_b64_tr_b16 v[252:253], v223 offset:32768\n"
    "ds_read_b64_tr_b16 a[238:239], v220 offset:38912\n"
    "ds_read_b64_tr_b16 v[230:231], v221 offset:38912\n"
    "ds_read_b64_tr_b16 v[254:255], v223 offset:38912\n"
    "ds_read_b128 v[16:19], v0 offset:0\n"
    "ds_read_b128 v[20:23], v1 offset:0\n"
    "ds_read_b128 v[24:27], v0 offset:2048\n"
    "ds_read_b128 v[28:31], v1 offset:2048\n"
    "ds_read_b128 v[32:35], v0 offset:4096\n"
    "ds_read_b128 v[36:39], v1 offset:4096\n"
    "s_waitcnt lgkmcnt(6)\n"
    "s_barrier\n"
    ".Lswin_loop%=:\n"
    "s_waitcnt lgkmcnt(0)\n"
    "v_mfma_f32_16x16x32_bf16 v[64:67], a[0:3], v[16:19], 0\n"
    "v_rcp_f32 v220, v52\n"
    "v_mfma_f32_16x16x32_bf16 v[64:67], a[4:7], v[20:23], v[64:67]\n"
    "v_mul_f32 v40, v220, v40\n"
    "v_mul_f32 v41, v220, v41\n"
    "v_mfma_f32_16x16x32_bf16 v[64:67], a[8:11], v[24:27], v[64:67]\n"
    "v_mul_f32 v42, v220, v42\n"
    "v_mul_f32 v43, v220, v43\n"
    "v_mfma_f32_16x16x32_bf16 v[64:67], a[12:15], v[28:31], v[64:67]\n"
    "v_cvt_pk_bf16_f32 v116, v40, v41\n"
    "v_cvt_pk_bf16_f32 v117, v42, v43\n"
    "v_mfma_f32_16x16x32_bf16 v[64:67], a[16:19], v[32:35], v[64:67]\n"
    "ds_read_b128 v[40:43], v0 offset:6144\n"
    "v_mul_f32 v44, v220, v44\n"
    "v_mfma_f32_16x16x32_bf16 v[64:67], a[20:23], v[36:39], v[64:67]\n"
    "v_mul_f32 v45, v220, v45\n"
    "v_mul_f32 v46, v220, v46\n"
    "v_mfma_f32_16x16x32_bf16 v[68:71], a[24:27], v[16:19], 0\n"
    "v_mul_f32 v47, v220, v47\n"
    "v_cvt_pk_bf16_f32 v118, v44, v45\n"
    "v_mfma_f32_16x16x32_bf16 v[68:71], a[28:31], v[20:23], v[68:71]\n"
    "v_cvt_pk_bf16_f32 v119, v46, v47\n"
    "ds_read_b128 v[44:47], v1 offset:6144\n"
    "v_mfma_f32_16x16x32_bf16 v[68:71], a[32:35], v[24:27], v[68:71]\n"
    "v_mul_f32 v64, s75, v64\n"
    "v_mul_f32 v65, s75, v65\n"
    "v_mfma_f32_16x16x32_bf16 v[68:71], a[36:39], v[28:31], v[68:71]\n"
    "v_mul_f32 v66, s75, v66\n"
    "v_mul_f32 v67, s75, v67\n"
    "v_mfma_f32_16x16x32_bf16 v[68:71], a[40:43], v[32:35], v[68:71]\n"
    "v_cvt_pk_bf16_f32 v76, v64, v65\n"
    "v_cvt_pk_bf16_f32 v77, v66, v67\n"
    "v_mfma_f32_16x16x32_bf16 v[68:71], a[44:47], v[36:39], v[68:71]\n"
    "v_mul_f32 v48, v220, v48\n"
    "v_mul_f32 v49, v220, v49\n"
    "v_mfma_f32_16x16x32_bf16 v[72:75], a[48:51], v[16:19], 0\n"
    "v_mul_f32 v50, v220, v50\n"
    "v_mul_f32 v51, v220, v51\n"
    "v_mfma_f32_16x16x32_bf16 v[72:75], a[52:55], v[20:23], v[72:75]\n"
    "v_cvt_pk_bf16_f32 v120, v48, v49\n"
    "v_cvt_pk_bf16_f32 v121, v50, v51\n"
    "v_mfma_f32_16x16x32_bf16 v[72:75], a[56:59], v[24:27], v[72:75]\n"
    "v_mul_f32 v68, s75, v68\n"
    "v_mul_f32 v69, s75, v69\n"
    "v_mfma_f32_16x16x32_bf16 v[72:75], a[60:63], v[28:31], v[72:75]\n"
    "v_mul_f32 v70, s75, v70\n"
    "v_mul_f32 v71, s75, v71\n"
    "v_mfma_f32_16x16x32_bf16 v[72:75], a[64:67], v[32:35], v[72:75]\n"
    "v_cvt_pk_bf16_f32 v78, v68, v69\n"
    "v_cvt_pk_bf16_f32 v79, v70, v71\n"
    "v_mfma_f32_16x16x32_bf16 v[72:75], a[68:71], v[36:39], v[72:75]\n"
    "ds_read_b128 v[48:51], v0 offset:8192\n"
    "s_add_u32 m0, s78, 0\n"
    "v_mfma_f32_16x16x32_bf16 v[64:67], a[72:75], v[16:19], 0\n"
    "ds_read_b128 v[52:55], v1 offset:8192\n"
    "s_mov_b32 s18, s69\n"
    "v_mfma_f32_16x16x32_bf16 v[64:67], a[76:79], v[20:23], v[64:67]\n"
    "ds_read_b128 v[56:59], v0 offset:10240\n"
    "s_min_u32 s26, s69, 6422528\n"
    "v_mfma_f32_16x16x32_bf16 v[64:67], a[80:83], v[24:27], v[64:67]\n"
    "v_mul_f32 v72, s75, v72\n"
    "v_mul_f32 v73, s75, v73\n"
    "v_mfma_f32_16x16x32_bf16 v[64:67], a[84:87], v[28:31], v[64:67]\n"
    "v_mul_f32 v74, s75, v74\n"
    "v_mul_f32 v75, s75, v75\n"
    "v_mfma_f32_16x16x32_bf16 v[64:67], a[88:91], v[32:35], v[64:67]\n"
    "v_cvt_pk_bf16_f32 v92, v72, v73\n"
    "v_cvt_pk_bf16_f32 v93, v74, v75\n"
    "v_mfma_f32_16x16x32_bf16 v[64:67], a[92:95], v[36:39], v[64:67]\n"
    "v_cvt_pk_bf16_f32 v98, v72, v73\n"
    "v_cvt_pk_bf16_f32 v99, v74, v75\n"
    "v_mfma_f32_16x16x32_bf16 v[68:71], a[96:99], v[16:19], 0\n"
    "ds_read_b128 v[60:63], v1 offset:10240\n"
    "s_mov_b32 s69, s70\n"
    "v_mfma_f32_16x16x32_bf16 v[68:71], a[100:103], v[20:23], v[68:71]\n"
    "ds_write_b128 v4, v[116:119] offset:0\n"
    "v_mfma_f32_16x16x32_bf16 v[68:71], a[104:107], v[24:27], v[68:71]\n"
    "v_cvt_pk_bf16_f32 v124, v64, v65\n"
    "v_cvt_pk_bf16_f32 v125, v66, v67\n"
    "v_mfma_f32_16x16x32_bf16 v[68:71], a[108:111], v[28:31], v[68:71]\n"
    "v_rcp_f32 v220, v136\n"
    "v_mfma_f32_16x16x32_bf16 v[68:71], a[112:115], v[32:35], v[68:71]\n"
    "v_mul_f32 v84, v220, v84\n"
    "v_mul_f32 v85, v220, v85\n"
    "v_mfma_f32_16x16x32_bf16 v[68:71], a[116:119], v[36:39], v[68:71]\n"
    "v_cvt_pk_bf16_f32 v116, v84, v85\n"
    "v_mul_f32 v86, v220, v86\n"
    "v_mfma_f32_16x16x32_bf16 v[72:75], a[120:123], v[16:19], 0\n"
    "ds_read_b128 v[16:19], v0 offset:12288\n"
    "v_mul_f32 v87, v220, v87\n"
    "v_mfma_f32_16x16x32_bf16 v[72:75], a[124:127], v[20:23], v[72:75]\n"
    "ds_read_b128 v[20:23], v1 offset:12288\n"
    "v_cvt_pk_bf16_f32 v117, v86, v87\n"
    "v_mfma_f32_16x16x32_bf16 v[72:75], a[128:131], v[24:27], v[72:75]\n"
    "v_cvt_pk_bf16_f32 v126, v68, v69\n"
    "v_cvt_pk_bf16_f32 v127, v70, v71\n"
    "v_mfma_f32_16x16x32_bf16 v[72:75], a[132:135], v[28:31], v[72:75]\n"
    "ds_read_b128 v[24:27], v0 offset:14336\n"
    "v_mul_f32 v132, v220, v132\n"
    "v_mfma_f32_16x16x32_bf16 v[72:75], a[136:139], v[32:35], v[72:75]\n"
    "ds_read_b128 v[28:31], v1 offset:14336\n"
    "v_mul_f32 v133, v220, v133\n"
    "v_mfma_f32_16x16x32_bf16 v[72:75], a[140:143], v[36:39], v[72:75]\n"
    "ds_read_b128 v[32:35], v0 offset:16384\n"
    "v_mul_f32 v134, v220, v134\n"
    "s_waitcnt lgkmcnt(5)\n"
    "v_mfma_f32_16x16x32_bf16 v[64:67], a[0:3], v[40:43], 0\n"
    "ds_read_b128 v[36:39], v1 offset:16384\n"
    "v_mul_f32 v135, v220, v135\n"
    "v_mfma_f32_16x16x32_bf16 v[64:67], a[4:7], v[44:47], v[64:67]\n"
    "v_rcp_f32 v221, v180\n"
    "v_mfma_f32_16x16x32_bf16 v[64:67], a[8:11], v[48:51], v[64:67]\n"
    "v_cvt_pk_bf16_f32 v140, v72, v73\n"
    "v_cvt_pk_bf16_f32 v141, v74, v75\n"
    "v_mfma_f32_16x16x32_bf16 v[64:67], a[12:15], v[52:55], v[64:67]\n"
    "v_mul_f32 v160, v221, v160\n"
    "v_mul_f32 v161, v221, v161\n"
    "v_mfma_f32_16x16x32_bf16 v[64:67], a[16:19], v[56:59], v[64:67]\n"
    "v_mul_f32 v162, v221, v162\n"
    "v_mul_f32 v163, v221, v163\n"
    "v_mfma_f32_16x16x32_bf16 v[64:67], a[20:23], v[60:63], v[64:67]\n"
    "v_mul_f32 v164, v221, v164\n"
    "v_mul_f32 v165, v221, v165\n"
    "v_mfma_f32_16x16x32_bf16 v[68:71], a[24:27], v[40:43], 0\n"
    "v_mul_f32 v166, v221, v166\n"
    "v_mul_f32 v167, v221, v167\n"
    "v_mfma_f32_16x16x32_bf16 v[68:71], a[28:31], v[44:47], v[68:71]\n"
    "v_cvt_pk_bf16_f32 v108, v160, v161\n"
    "v_cvt_pk_bf16_f32 v109, v162, v163\n"
    "v_mfma_f32_16x16x32_bf16 v[68:71], a[32:35], v[48:51], v[68:71]\n"
    "v_mul_f32 v64, s75, v64\n"
    "v_mul_f32 v65, s75, v65\n"
    "v_mfma_f32_16x16x32_bf16 v[68:71], a[36:39], v[52:55], v[68:71]\n"
    "v_mul_f32 v66, s75, v66\n"
    "v_mul_f32 v67, s75, v67\n"
    "v_mfma_f32_16x16x32_bf16 v[68:71], a[40:43], v[56:59], v[68:71]\n"
    "v_cvt_pk_bf16_f32 v80, v64, v65\n"
    "v_cvt_pk_bf16_f32 v81, v66, v67\n"
    "v_mfma_f32_16x16x32_bf16 v[68:71], a[44:47], v[60:63], v[68:71]\n"
    "v_cvt_pk_bf16_f32 v110, v164, v165\n"
    "v_cvt_pk_bf16_f32 v111, v166, v167\n"
    "v_mfma_f32_16x16x32_bf16 v[72:75], a[48:51], v[40:43], 0\n"
    "ds_write_b128 v4, v[108:111] offset:6144\n"
    "v_mfma_f32_16x16x32_bf16 v[72:75], a[52:55], v[44:47], v[72:75]\n"
    "v_mul_f32 v176, v221, v176\n"
    "v_mul_f32 v177, v221, v177\n"
    "v_mfma_f32_16x16x32_bf16 v[72:75], a[56:59], v[48:51], v[72:75]\n"
    "v_mul_f32 v68, s75, v68\n"
    "v_mul_f32 v69, s75, v69\n"
    "v_mfma_f32_16x16x32_bf16 v[72:75], a[60:63], v[52:55], v[72:75]\n"
    "v_mul_f32 v70, s75, v70\n"
    "v_mul_f32 v71, s75, v71\n"
    "v_mfma_f32_16x16x32_bf16 v[72:75], a[64:67], v[56:59], v[72:75]\n"
    "v_cvt_pk_bf16_f32 v82, v68, v69\n"
    "v_cvt_pk_bf16_f32 v83, v70, v71\n"
    "v_mfma_f32_16x16x32_bf16 v[72:75], a[68:71], v[60:63], v[72:75]\n"
    "v_mul_f32 v178, v221, v178\n"
    "v_mul_f32 v179, v221, v179\n"
    "v_mfma_f32_16x16x32_bf16 v[64:67], a[72:75], v[40:43], 0\n"
    "v_rcp_f32 v221, v212\n"
    "v_mfma_f32_16x16x32_bf16 v[64:67], a[76:79], v[44:47], v[64:67]\n"
    "v_mul_f32 v192, v221, v192\n"
    "v_mul_f32 v193, v221, v193\n"
    "v_mfma_f32_16x16x32_bf16 v[64:67], a[80:83], v[48:51], v[64:67]\n"
    "v_mul_f32 v72, s75, v72\n"
    "v_mul_f32 v73, s75, v73\n"
    "v_mfma_f32_16x16x32_bf16 v[64:67], a[84:87], v[52:55], v[64:67]\n"
    "v_mul_f32 v74, s75, v74\n"
    "v_mul_f32 v75, s75, v75\n"
    "v_mfma_f32_16x16x32_bf16 v[64:67], a[88:91], v[56:59], v[64:67]\n"
    "v_cvt_pk_bf16_f32 v100, v72, v73\n"
    "v_cvt_pk_bf16_f32 v101, v74, v75\n"
    "v_mfma_f32_16x16x32_bf16 v[64:67], a[92:95], v[60:63], v[64:67]\n"
    "v_cvt_pk_bf16_f32 v106, v72, v73\n"
    "v_cvt_pk_bf16_f32 v107, v74, v75\n"
    "v_mfma_f32_16x16x32_bf16 v[68:71], a[96:99], v[40:43], 0\n"
    "v_mul_f32 v194, v221, v194\n"
    "v_mul_f32 v195, v221, v195\n"
    "v_mfma_f32_16x16x32_bf16 v[68:71], a[100:103], v[44:47], v[68:71]\n"
    "v_mul_f32 v196, v221, v196\n"
    "v_mul_f32 v197, v221, v197\n"
    "v_mfma_f32_16x16x32_bf16 v[68:71], a[104:107], v[48:51], v[68:71]\n"
    "v_cvt_pk_bf16_f32 v128, v64, v65\n"
    "v_cvt_pk_bf16_f32 v129, v66, v67\n"
    "v_mfma_f32_16x16x32_bf16 v[68:71], a[108:111], v[52:55], v[68:71]\n"
    "v_mul_f32 v198, v221, v198\n"
    "v_mul_f32 v199, v221, v199\n"
    "v_mfma_f32_16x16x32_bf16 v[68:71], a[112:115], v[56:59], v[68:71]\n"
    "v_cvt_pk_bf16_f32 v108, v192, v193\n"
    "v_cvt_pk_bf16_f32 v109, v194, v195\n"
    "v_mfma_f32_16x16x32_bf16 v[68:71], a[116:119], v[60:63], v[68:71]\n"
    "v_cvt_pk_bf16_f32 v110, v196, v197\n"
    "v_cvt_pk_bf16_f32 v111, v198, v199\n"
    "v_mfma_f32_16x16x32_bf16 v[72:75], a[120:123], v[40:43], 0\n"
    "ds_write_b128 v4, v[108:111] offset:18432\n"
    "v_mfma_f32_16x16x32_bf16 v[72:75], a[124:127], v[44:47], v[72:75]\n"
    "ds_read_b128 v[40:43], v0 offset:18432\n"
    "v_mul_f32 v88, v220, v88\n"
    "v_mfma_f32_16x16x32_bf16 v[72:75], a[128:131], v[48:51], v[72:75]\n"
    "v_cvt_pk_bf16_f32 v130, v68, v69\n"
    "v_cvt_pk_bf16_f32 v131, v70, v71\n"
    "v_mfma_f32_16x16x32_bf16 v[72:75], a[132:135], v[52:55], v[72:75]\n"
    "ds_read_b128 v[44:47], v1 offset:18432\n"
    "v_mul_f32 v89, v220, v89\n"
    "v_mfma_f32_16x16x32_bf16 v[72:75], a[136:139], v[56:59], v[72:75]\n"
    "buffer_load_dwordx4 v6, s[16:19], s65 offen nt lds\n"
    "v_mfma_f32_16x16x32_bf16 v[72:75], a[140:143], v[60:63], v[72:75]\n"
    "ds_read_b128 v[48:51], v0 offset:20480\n"
    "s_add_u32 m0, s78, 1024\n"
    "s_waitcnt lgkmcnt(4)\n"
    "v_mfma_f32_16x16x32_bf16 v[64:67], a[0:3], v[16:19], 0\n"
    "ds_read_b128 v[52:55], v1 offset:20480\n"
    "v_cvt_pk_bf16_f32 v118, v88, v89\n"
    "v_mfma_f32_16x16x32_bf16 v[64:67], a[4:7], v[20:23], v[64:67]\n"
    "ds_write_b64 v5, v[120:121] offset:0\n"
    "v_mfma_f32_16x16x32_bf16 v[64:67], a[8:11], v[24:27], v[64:67]\n"
    "v_cvt_pk_bf16_f32 v144, v72, v73\n"
    "v_cvt_pk_bf16_f32 v145, v74, v75\n"
    "v_mfma_f32_16x16x32_bf16 v[64:67], a[12:15], v[28:31], v[64:67]\n"
    "buffer_load_dwordx4 v7, s[16:19], s65 offen nt lds\n"
    "v_mfma_f32_16x16x32_bf16 v[64:67], a[16:19], v[32:35], v[64:67]\n"
    "s_add_u32 m0, s78, 1920\n"
    "ds_read_b128 v[56:59], v0 offset:22528\n"
    "v_mfma_f32_16x16x32_bf16 v[64:67], a[20:23], v[36:39], v[64:67]\n"
    "v_cvt_pk_bf16_f32 v120, v132, v133\n"
    "ds_read_b128 v[60:63], v1 offset:22528\n"
    "v_mfma_f32_16x16x32_bf16 v[68:71], a[24:27], v[16:19], 0\n"
    "v_cvt_pk_bf16_f32 v121, v134, v135\n"
    "v_cvt_pk_bf16_f32 v112, v176, v177\n"
    "v_mfma_f32_16x16x32_bf16 v[68:71], a[28:31], v[20:23], v[68:71]\n"
    "buffer_load_dwordx4 v6, s[16:19], s65 offen offset:128 nt lds\n"
    "v_mfma_f32_16x16x32_bf16 v[68:71], a[32:35], v[24:27], v[68:71]\n"
    "v_mul_f32 v64, s75, v64\n"
    "v_mul_f32 v65, s75, v65\n"
    "v_mfma_f32_16x16x32_bf16 v[68:71], a[36:39], v[28:31], v[68:71]\n"
    "v_mul_f32 v66, s75, v66\n"
    "v_mul_f32 v67, s75, v67\n"
    "v_mfma_f32_16x16x32_bf16 v[68:71], a[40:43], v[32:35], v[68:71]\n"
    "v_cvt_pk_bf16_f32 v84, v64, v65\n"
    "v_cvt_pk_bf16_f32 v85, v66, v67\n"
    "v_mfma_f32_16x16x32_bf16 v[68:71], a[44:47], v[36:39], v[68:71]\n"
    "s_add_u32 m0, s78, 2944\n"
    "v_cvt_pk_bf16_f32 v113, v178, v179\n"
    "v_mfma_f32_16x16x32_bf16 v[72:75], a[48:51], v[16:19], 0\n"
    "buffer_load_dwordx4 v7, s[16:19], s65 offen offset:128 nt lds\n"
    "v_mfma_f32_16x16x32_bf16 v[72:75], a[52:55], v[20:23], v[72:75]\n"
    "s_add_u32 m0, s78, 3840\n"
    "v_mul_f32 v90, v220, v90\n"
    "v_mfma_f32_16x16x32_bf16 v[72:75], a[56:59], v[24:27], v[72:75]\n"
    "v_mul_f32 v68, s75, v68\n"
    "v_mul_f32 v69, s75, v69\n"
    "v_mfma_f32_16x16x32_bf16 v[72:75], a[60:63], v[28:31], v[72:75]\n"
    "v_mul_f32 v70, s75, v70\n"
    "v_mul_f32 v71, s75, v71\n"
    "v_mfma_f32_16x16x32_bf16 v[72:75], a[64:67], v[32:35], v[72:75]\n"
    "v_cvt_pk_bf16_f32 v86, v68, v69\n"
    "v_cvt_pk_bf16_f32 v87, v70, v71\n"
    "v_mfma_f32_16x16x32_bf16 v[72:75], a[68:71], v[36:39], v[72:75]\n"
    "buffer_load_dwordx4 v6, s[16:19], s65 offen offset:256 nt lds\n"
    "v_mfma_f32_16x16x32_bf16 v[64:67], a[72:75], v[16:19], 0\n"
    "s_add_u32 m0, s78, 4864\n"
    "v_mul_f32 v91, v220, v91\n"
    "v_mfma_f32_16x16x32_bf16 v[64:67], a[76:79], v[20:23], v[64:67]\n"
    "ds_write_b64 v5, v[112:113] offset:6144\n"
    "v_mfma_f32_16x16x32_bf16 v[64:67], a[80:83], v[24:27], v[64:67]\n"
    "buffer_load_dwordx4 v7, s[16:19], s65 offen offset:256 nt lds\n"
    "v_mfma_f32_16x16x32_bf16 v[64:67], a[84:87], v[28:31], v[64:67]\n"
    "v_mul_f32 v72, s75, v72\n"
    "v_mul_f32 v73, s75, v73\n"
    "v_mfma_f32_16x16x32_bf16 v[64:67], a[88:91], v[32:35], v[64:67]\n"
    "v_mul_f32 v74, s75, v74\n"
    "v_mul_f32 v75, s75, v75\n"
    "v_mfma_f32_16x16x32_bf16 v[64:67], a[92:95], v[36:39], v[64:67]\n"
    "v_cvt_pk_bf16_f32 v108, v72, v73\n"
    "v_cvt_pk_bf16_f32 v109, v74, v75\n"
    "v_mfma_f32_16x16x32_bf16 v[68:71], a[96:99], v[16:19], 0\n"
    "v_cvt_pk_bf16_f32 v114, v72, v73\n"
    "v_cvt_pk_bf16_f32 v115, v74, v75\n"
    "v_mfma_f32_16x16x32_bf16 v[68:71], a[100:103], v[20:23], v[68:71]\n"
    "s_add_u32 m0, s79, 0\n"
    "v_cvt_pk_bf16_f32 v119, v90, v91\n"
    "v_mfma_f32_16x16x32_bf16 v[68:71], a[104:107], v[24:27], v[68:71]\n"
    "v_cvt_pk_bf16_f32 v132, v64, v65\n"
    "v_cvt_pk_bf16_f32 v133, v66, v67\n"
    "v_mfma_f32_16x16x32_bf16 v[68:71], a[108:111], v[28:31], v[68:71]\n"
    "buffer_load_dwordx4 v8, s[24:27], s72 offen nt lds\n"
    "v_mfma_f32_16x16x32_bf16 v[68:71], a[112:115], v[32:35], v[68:71]\n"
    "s_add_u32 m0, s79, 1024\n"
    "v_mul_f32 v208, v221, v208\n"
    "v_mfma_f32_16x16x32_bf16 v[68:71], a[116:119], v[36:39], v[68:71]\n"
    "v_mul_f32 v209, v221, v209\n"
    "v_mul_f32 v210, v221, v210\n"
    "v_mfma_f32_16x16x32_bf16 v[72:75], a[120:123], v[16:19], 0\n"
    "v_mul_f32 v211, v221, v211\n"
    "v_cvt_pk_bf16_f32 v112, v208, v209\n"
    "v_mfma_f32_16x16x32_bf16 v[72:75], a[124:127], v[20:23], v[72:75]\n"
    "buffer_load_dwordx4 v9, s[24:27], s72 offen nt lds\n"
    "v_mfma_f32_16x16x32_bf16 v[72:75], a[128:131], v[24:27], v[72:75]\n"
    "v_cvt_pk_bf16_f32 v134, v68, v69\n"
    "v_cvt_pk_bf16_f32 v135, v70, v71\n"
    "v_mfma_f32_16x16x32_bf16 v[72:75], a[132:135], v[28:31], v[72:75]\n"
    "v_cvt_pk_bf16_f32 v113, v210, v211\n"
    "ds_read_b128 v[152:155], v3 offset:0\n"
    "v_mfma_f32_16x16x32_bf16 v[72:75], a[136:139], v[32:35], v[72:75]\n"
    "ds_write_b128 v4, v[116:119] offset:12288\n"
    "v_mfma_f32_16x16x32_bf16 v[72:75], a[140:143], v[36:39], v[72:75]\n"
    "ds_write_b64 v5, v[120:121] offset:12288\n"
    "s_waitcnt lgkmcnt(3)\n"
    "v_mfma_f32_16x16x32_bf16 v[64:67], a[0:3], v[40:43], 0\n"
    "ds_write_b64 v5, v[112:113] offset:18432\n"
    "v_mfma_f32_16x16x32_bf16 v[64:67], a[4:7], v[44:47], v[64:67]\n"
    "ds_read_b128 v[156:159], v3 offset:1024\n"
    "s_mov_b32 s65, s66\n"
    "v_mfma_f32_16x16x32_bf16 v[64:67], a[8:11], v[48:51], v[64:67]\n"
    "ds_read_b128 v[168:171], v3 offset:4096\n"
    "v_cvt_pk_bf16_f32 v146, v72, v73\n"
    "v_mfma_f32_16x16x32_bf16 v[64:67], a[12:15], v[52:55], v[64:67]\n"
    "v_cvt_pk_bf16_f32 v147, v74, v75\n"
    "ds_read_b128 v[172:175], v3 offset:5120\n"
    "v_mfma_f32_16x16x32_bf16 v[64:67], a[16:19], v[56:59], v[64:67]\n"
    "ds_read_b128 v[184:187], v3 offset:8192\n"
    "s_mov_b32 s70, s71\n"
    "v_mfma_f32_16x16x32_bf16 v[64:67], a[20:23], v[60:63], v[64:67]\n"
    "ds_read_b128 v[188:191], v3 offset:9216\n"
    "s_xor_b32 s78, s78, 32768\n"
    "v_mfma_f32_16x16x32_bf16 v[68:71], a[24:27], v[40:43], 0\n"
    "ds_read_b128 v[160:163], v3 offset:2048\n"
    "s_xor_b32 s79, s79, 32768\n"
    "v_mfma_f32_16x16x32_bf16 v[68:71], a[28:31], v[44:47], v[68:71]\n"
    "s_waitcnt lgkmcnt(6)\n"
    "s_barrier\n"
    "v_mfma_f32_16x16x32_bf16 v[68:71], a[32:35], v[48:51], v[68:71]\n"
    "v_mul_f32 v64, s75, v64\n"
    "v_mul_f32 v65, s75, v65\n"
    "v_mfma_f32_16x16x32_bf16 v[68:71], a[36:39], v[52:55], v[68:71]\n"
    "v_mul_f32 v66, s75, v66\n"
    "v_mul_f32 v67, s75, v67\n"
    "v_mfma_f32_16x16x32_bf16 v[68:71], a[40:43], v[56:59], v[68:71]\n"
    "v_cvt_pk_bf16_f32 v88, v64, v65\n"
    "v_cvt_pk_bf16_f32 v89, v66, v67\n"
    "v_mfma_f32_16x16x32_bf16 v[68:71], a[44:47], v[60:63], v[68:71]\n"
    "ds_read_b128 v[94:97], v0 offset:24576\n"
    "s_mov_b32 s72, s1\n"
    "v_mfma_f32_16x16x32_bf16 v[72:75], a[48:51], v[40:43], 0\n"
    "ds_read_b128 v[102:105], v0 offset:26624\n"
    "s_mov_b32 s66, s67\n"
    "v_mfma_f32_16x16x32_bf16 v[72:75], a[52:55], v[44:47], v[72:75]\n"
    "ds_read_b128 v[110:113], v0 offset:28672\n"
    "s_mov_b32 s71, 0\n"
    "v_mfma_f32_16x16x32_bf16 v[72:75], a[56:59], v[48:51], v[72:75]\n"
    "v_mul_f32 v68, s75, v68\n"
    "v_mul_f32 v69, s75, v69\n"
    "v_mfma_f32_16x16x32_bf16 v[72:75], a[60:63], v[52:55], v[72:75]\n"
    "v_mul_f32 v70, s75, v70\n"
    "v_mul_f32 v71, s75, v71\n"
    "v_mfma_f32_16x16x32_bf16 v[72:75], a[64:67], v[56:59], v[72:75]\n"
    "v_cvt_pk_bf16_f32 v90, v68, v69\n"
    "v_cvt_pk_bf16_f32 v91, v70, v71\n"
    "v_mfma_f32_16x16x32_bf16 v[72:75], a[68:71], v[60:63], v[72:75]\n"
    "ds_read_b128 v[164:167], v3 offset:3072\n"
    "v_mfma_f32_16x16x32_bf16 v[64:67], a[72:75], v[40:43], 0\n"
    "ds_read_b128 v[200:203], v3 offset:12288\n"
    "v_mfma_f32_16x16x32_bf16 v[64:67], a[76:79], v[44:47], v[64:67]\n"
    "ds_read_b128 v[118:121], v0 offset:30720\n"
    "v_mfma_f32_16x16x32_bf16 v[64:67], a[80:83], v[48:51], v[64:67]\n"
    "ds_read_b128 v[204:207], v3 offset:13312\n"
    "v_mfma_f32_16x16x32_bf16 v[64:67], a[84:87], v[52:55], v[64:67]\n"
    "v_mul_f32 v72, s75, v72\n"
    "v_mul_f32 v73, s75, v73\n"
    "v_mfma_f32_16x16x32_bf16 v[64:67], a[88:91], v[56:59], v[64:67]\n"
    "v_mul_f32 v74, s75, v74\n"
    "v_mul_f32 v75, s75, v75\n"
    "v_mfma_f32_16x16x32_bf16 v[64:67], a[92:95], v[60:63], v[64:67]\n"
    "v_cvt_pk_bf16_f32 v116, v72, v73\n"
    "v_cvt_pk_bf16_f32 v117, v74, v75\n"
    "v_mfma_f32_16x16x32_bf16 v[68:71], a[96:99], v[40:43], 0\n"
    "v_cvt_pk_bf16_f32 v122, v72, v73\n"
    "v_cvt_pk_bf16_f32 v123, v74, v75\n"
    "v_mfma_f32_16x16x32_bf16 v[68:71], a[100:103], v[44:47], v[68:71]\n"
    "ds_read_b128 v[176:179], v3 offset:6144\n"
    "v_mfma_f32_16x16x32_bf16 v[68:71], a[104:107], v[48:51], v[68:71]\n"
    "v_cvt_pk_bf16_f32 v136, v64, v65\n"
    "v_cvt_pk_bf16_f32 v137, v66, v67\n"
    "v_mfma_f32_16x16x32_bf16 v[68:71], a[108:111], v[52:55], v[68:71]\n"
    "ds_read_b128 v[180:183], v3 offset:7168\n"
    "v_mfma_f32_16x16x32_bf16 v[68:71], a[112:115], v[56:59], v[68:71]\n"
    "ds_read_b128 v[192:195], v3 offset:10240\n"
    "v_mfma_f32_16x16x32_bf16 v[68:71], a[116:119], v[60:63], v[68:71]\n"
    "ds_read_b128 v[196:199], v3 offset:11264\n"
    "v_mfma_f32_16x16x32_bf16 v[72:75], a[120:123], v[40:43], 0\n"
    "ds_read_b128 v[208:211], v3 offset:14336\n"
    "v_mfma_f32_16x16x32_bf16 v[72:75], a[124:127], v[44:47], v[72:75]\n"
    "ds_read_b128 v[212:215], v3 offset:15360\n"
    "v_mfma_f32_16x16x32_bf16 v[72:75], a[128:131], v[48:51], v[72:75]\n"
    "v_mfma_f32_16x16x32_bf16 v[72:75], a[132:135], v[52:55], v[72:75]\n"
    "s_nop 0\n"
    "v_cvt_pk_bf16_f32 v138, v68, v69\n"
    "v_mfma_f32_16x16x32_bf16 v[72:75], a[136:139], v[56:59], v[72:75]\n"
    "v_cvt_pk_bf16_f32 v139, v70, v71\n"
    "v_mfma_f32_16x16x32_bf16 v[72:75], a[140:143], v[60:63], v[72:75]\n"
    "s_waitcnt lgkmcnt(5)\n"
    "v_mfma_f32_16x16x32_bf16 v[152:155], v[140:143], v[92:95], v[152:155]\n"
    "v_mfma_f32_16x16x32_bf16 v[152:155], v[124:127], v[76:79], v[152:155]\n"
    "v_mfma_f32_16x16x32_bf16 v[156:159], v[142:145], v[96:99], v[156:159]\n"
    "ds_read_b128 v[94:97], v1 offset:24576\n"
    "v_mfma_f32_16x16x32_bf16 v[156:159], v[128:131], v[76:79], v[156:159]\n"
    "v_mfma_f32_16x16x32_bf16 v[168:171], v[140:143], v[100:103], v[168:171]\n"
    "v_mfma_f32_16x16x32_bf16 v[168:171], v[124:127], v[80:83], v[168:171]\n"
    "s_nop 0\n"
    "v_cvt_pk_bf16_f32 v150, v72, v73\n"
    "v_mfma_f32_16x16x32_bf16 v[172:175], v[142:145], v[104:107], v[172:175]\n"
    "v_cvt_pk_bf16_f32 v151, v74, v75\n"
    "ds_read_b128 v[102:105], v1 offset:26624\n"
    "v_mfma_f32_16x16x32_bf16 v[172:175], v[128:131], v[80:83], v[172:175]\n"
    "v_max3_f32 v216, v152, v153, v154\n"
    "v_max3_f32 v216, v216, v155, v156\n"
    "v_mfma_f32_16x16x32_bf16 v[184:187], v[140:143], v[108:111], v[184:187]\n"
    "v_max3_f32 v217, v168, v169, v170\n"
    "v_max3_f32 v216, v216, v157, v158\n"
    "v_mfma_f32_16x16x32_bf16 v[184:187], v[124:127], v[84:87], v[184:187]\n"
    "v_mfma_f32_16x16x32_bf16 v[188:191], v[142:145], v[112:115], v[188:191]\n"
    "ds_read_b128 v[110:113], v1 offset:28672\n"
    "v_max3_f32 v217, v217, v171, v172\n"
    "v_mfma_f32_16x16x32_bf16 v[188:191], v[128:131], v[84:87], v[188:191]\n"
    "v_max3_f32 v217, v217, v173, v174\n"
    "s_waitcnt lgkmcnt(2)\n"
    "v_mfma_f32_16x16x32_bf16 v[160:163], v[146:149], v[92:95], v[160:163]\n"
    "v_mfma_f32_16x16x32_bf16 v[160:163], v[132:135], v[76:79], v[160:163]\n"
    "s_nop 0\n"
    "v_max3_f32 v218, v184, v185, v186\n"
    "v_mfma_f32_16x16x32_bf16 v[164:167], v[148:151], v[96:99], v[164:167]\n"
    "v_mfma_f32_16x16x32_bf16 v[164:167], v[136:139], v[76:79], v[164:167]\n"
    "s_nop 0\n"
    "v_max3_f32 v218, v218, v187, v188\n"
    "v_mfma_f32_16x16x32_bf16 v[200:203], v[140:143], v[116:119], v[200:203]\n"
    "v_max3_f32 v218, v218, v189, v190\n"
    "v_max3_f32 v216, v216, v159, v160\n"
    "v_mfma_f32_16x16x32_bf16 v[200:203], v[124:127], v[88:91], v[200:203]\n"
    "v_max3_f32 v216, v216, v161, v162\n"
    "v_mfma_f32_16x16x32_bf16 v[204:207], v[142:145], v[120:123], v[204:207]\n"
    "ds_read_b128 v[118:121], v1 offset:30720\n"
    "v_max3_f32 v216, v216, v163, v164\n"
    "v_mfma_f32_16x16x32_bf16 v[204:207], v[128:131], v[88:91], v[204:207]\n"
    "v_max3_f32 v216, v216, v165, v166\n"
    "v_max_f32 v216, v216, v167\n"
    "s_waitcnt lgkmcnt(1)\n"
    "v_mfma_f32_16x16x32_bf16 v[176:179], v[146:149], v[100:103], v[176:179]\n"
    "v_max3_f32 v219, v200, v201, v202\n"
    "v_mfma_f32_16x16x32_bf16 v[176:179], v[132:135], v[80:83], v[176:179]\n"
    "v_mfma_f32_16x16x32_bf16 v[180:183], v[148:151], v[104:107], v[180:183]\n"
    "v_mfma_f32_16x16x32_bf16 v[180:183], v[136:139], v[80:83], v[180:183]\n"
    "s_nop 0\n"
    "v_max3_f32 v219, v219, v203, v204\n"
    "v_mfma_f32_16x16x32_bf16 v[192:195], v[146:149], v[108:111], v[192:195]\n"
    "v_max3_f32 v219, v219, v205, v206\n"
    "v_mfma_f32_16x16x32_bf16 v[192:195], v[132:135], v[84:87], v[192:195]\n"
    "s_nop 0\n"
    "v_max3_f32 v217, v217, v175, v176\n"
    "v_mfma_f32_16x16x32_bf16 v[196:199], v[148:151], v[112:115], v[196:199]\n"
    "v_max3_f32 v217, v217, v177, v178\n"
    "v_max3_f32 v217, v217, v179, v180\n"
    "v_mfma_f32_16x16x32_bf16 v[196:199], v[136:139], v[84:87], v[196:199]\n"
    "v_max3_f32 v217, v217, v181, v182\n"
    "v_max_f32 v217, v217, v183\n"
    "s_waitcnt lgkmcnt(0)\n"
    "v_mfma_f32_16x16x32_bf16 v[208:211], v[146:149], v[116:119], v[208:211]\n"
    "v_max3_f32 v218, v218, v191, v192\n"
    "v_permlane32_swap_b32 v216, v217\n"
    "v_mfma_f32_16x16x32_bf16 v[208:211], v[132:135], v[88:91], v[208:211]\n"
    "v_max_f32 v216, v216, v217\n"
    "v_mov_b32 v217, v216\n"
    "v_mfma_f32_16x16x32_bf16 v[212:215], v[148:151], v[120:123], v[212:215]\n"
    "v_max3_f32 v218, v218, v193, v194\n"
    "v_permlane16_swap_b32 v216, v217\n"
    "v_mfma_f32_16x16x32_bf16 v[212:215], v[136:139], v[88:91], v[212:215]\n"
    "v_max_f32 v216, v216, v217\n"
    "v_mul_f32 v216, s74, v216\n"
    "v_mfma_f32_16x16x32_bf16 v[64:67], v[16:19], a[144:147], 0\n"
    "v_mov_b32 v217, v216\n"
    "v_max3_f32 v219, v219, v207, v208\n"
    "v_mfma_f32_16x16x32_bf16 v[64:67], v[20:23], a[148:151], v[64:67]\n"
    "v_permlane32_swap_b32 v216, v217\n"
    "v_fma_f32 v152, v152, s73, v216\n"
    "v_mfma_f32_16x16x32_bf16 v[64:67], v[24:27], a[152:155], v[64:67]\n"
    "v_fma_f32 v153, v153, s73, v216\n"
    "v_fma_f32 v154, v154, s73, v216\n"
    "v_mfma_f32_16x16x32_bf16 v[64:67], v[28:31], a[156:159], v[64:67]\n"
    "v_exp_f32 v152, v152\n"
    "v_mfma_f32_16x16x32_bf16 v[64:67], v[32:35], a[160:163], v[64:67]\n"
    "v_exp_f32 v153, v153\n"
    "v_mfma_f32_16x16x32_bf16 v[64:67], v[36:39], a[164:167], v[64:67]\n"
    "v_fma_f32 v155, v155, s73, v216\n"
    "v_fma_f32 v156, v156, s73, v216\n"
    "v_mfma_f32_16x16x32_bf16 v[68:71], v[16:19], a[168:171], 0\n"
    "v_exp_f32 v154, v154\n"
    "v_mfma_f32_16x16x32_bf16 v[68:71], v[20:23], a[172:175], v[68:71]\n"
    "v_exp_f32 v155, v155\n"
    "v_mfma_f32_16x16x32_bf16 v[68:71], v[24:27], a[176:179], v[68:71]\n"
    "v_fma_f32 v157, v157, s73, v216\n"
    "v_cvt_pk_bf16_f32 v80, v64, v65\n"
    "v_mfma_f32_16x16x32_bf16 v[68:71], v[28:31], a[180:183], v[68:71]\n"
    "v_cvt_pk_bf16_f32 v81, v66, v67\n"
    "v_fma_f32 v158, v158, s73, v216\n"
    "v_mfma_f32_16x16x32_bf16 v[68:71], v[32:35], a[184:187], v[68:71]\n"
    "v_fma_f32 v159, v159, s73, v216\n"
    "v_cvt_pk_bf16_f32 v152, v152, v153\n"
    "v_mfma_f32_16x16x32_bf16 v[68:71], v[36:39], a[188:191], v[68:71]\n"
    "v_exp_f32 v156, v156\n"
    "v_mfma_f32_16x16x32_bf16 v[72:75], v[16:19], a[192:195], 0\n"
    "ds_read_b128 v[16:19], v0 offset:0\n"
    "v_cvt_pk_bf16_f32 v153, v154, v155\n"
    "v_mfma_f32_16x16x32_bf16 v[72:75], v[20:23], a[196:199], v[72:75]\n"
    "ds_read_b128 v[20:23], v1 offset:0\n"
    "v_fma_f32 v160, v160, s73, v216\n"
    "v_mfma_f32_16x16x32_bf16 v[72:75], v[24:27], a[200:203], v[72:75]\n"
    "v_cvt_pk_bf16_f32 v128, v68, v69\n"
    "v_cvt_pk_bf16_f32 v129, v70, v71\n"
    "v_mfma_f32_16x16x32_bf16 v[72:75], v[28:31], a[204:207], v[72:75]\n"
    "ds_read_b128 v[24:27], v0 offset:2048\n"
    "v_fma_f32 v161, v161, s73, v216\n"
    "v_mfma_f32_16x16x32_bf16 v[72:75], v[32:35], a[208:211], v[72:75]\n"
    "ds_read_b128 v[28:31], v1 offset:2048\n"
    "v_fma_f32 v162, v162, s73, v216\n"
    "v_mfma_f32_16x16x32_bf16 v[72:75], v[36:39], a[212:215], v[72:75]\n"
    "ds_read_b128 v[32:35], v0 offset:4096\n"
    "v_fma_f32 v163, v163, s73, v216\n"
    "v_mfma_f32_16x16x32_bf16 v[64:67], v[40:43], a[144:147], 0\n"
    "ds_read_b128 v[36:39], v1 offset:4096\n"
    "v_fma_f32 v164, v164, s73, v216\n"
    "v_mfma_f32_16x16x32_bf16 v[64:67], v[44:47], a[148:151], v[64:67]\n"
    "v_exp_f32 v157, v157\n"
    "v_mfma_f32_16x16x32_bf16 v[64:67], v[48:51], a[152:155], v[64:67]\n"
    "v_cvt_pk_bf16_f32 v96, v72, v73\n"
    "v_cvt_pk_bf16_f32 v97, v74, v75\n"
    "v_mfma_f32_16x16x32_bf16 v[64:67], v[52:55], a[156:159], v[64:67]\n"
    "v_exp_f32 v158, v158\n"
    "v_mfma_f32_16x16x32_bf16 v[64:67], v[56:59], a[160:163], v[64:67]\n"
    "v_exp_f32 v159, v159\n"
    "v_mfma_f32_16x16x32_bf16 v[64:67], v[60:63], a[164:167], v[64:67]\n"
    "v_cvt_pk_bf16_f32 v154, v156, v157\n"
    "v_cvt_pk_bf16_f32 v155, v158, v159\n"
    "v_mfma_f32_16x16x32_bf16 v[68:71], v[40:43], a[168:171], 0\n"
    "v_fma_f32 v165, v165, s73, v216\n"
    "v_fma_f32 v166, v166, s73, v216\n"
    "v_mfma_f32_16x16x32_bf16 v[68:71], v[44:47], a[172:175], v[68:71]\n"
    "v_fma_f32 v167, v167, s73, v216\n"
    "v_fma_f32 v168, v168, s73, v217\n"
    "v_mfma_f32_16x16x32_bf16 v[68:71], v[48:51], a[176:179], v[68:71]\n"
    "v_cvt_pk_bf16_f32 v82, v64, v65\n"
    "v_cvt_pk_bf16_f32 v83, v66, v67\n"
    "v_mfma_f32_16x16x32_bf16 v[68:71], v[52:55], a[180:183], v[68:71]\n"
    "v_exp_f32 v160, v160\n"
    "v_mfma_f32_16x16x32_bf16 v[68:71], v[56:59], a[184:187], v[68:71]\n"
    "v_exp_f32 v161, v161\n"
    "v_mfma_f32_16x16x32_bf16 v[68:71], v[60:63], a[188:191], v[68:71]\n"
    "v_exp_f32 v162, v162\n"
    "v_mfma_f32_16x16x32_bf16 v[72:75], v[40:43], a[192:195], 0\n"
    "ds_read_b128 v[40:43], v0 offset:6144\n"
    "v_cvt_pk_bf16_f32 v156, v160, v161\n"
    "v_mfma_f32_16x16x32_bf16 v[72:75], v[44:47], a[196:199], v[72:75]\n"
    "ds_read_b128 v[44:47], v1 offset:6144\n"
    "v_fma_f32 v169, v169, s73, v217\n"
    "v_mfma_f32_16x16x32_bf16 v[72:75], v[48:51], a[200:203], v[72:75]\n"
    "v_cvt_pk_bf16_f32 v130, v68, v69\n"
    "v_cvt_pk_bf16_f32 v131, v70, v71\n"
    "v_mfma_f32_16x16x32_bf16 v[72:75], v[52:55], a[204:207], v[72:75]\n"
    "ds_read_b128 v[48:51], v0 offset:8192\n"
    "v_fma_f32 v170, v170, s73, v217\n"
    "v_mfma_f32_16x16x32_bf16 v[72:75], v[56:59], a[208:211], v[72:75]\n"
    "ds_read_b128 v[52:55], v1 offset:8192\n"
    "v_fma_f32 v171, v171, s73, v217\n"
    "v_mfma_f32_16x16x32_bf16 v[72:75], v[60:63], a[212:215], v[72:75]\n"
    "ds_read_b128 v[56:59], v0 offset:10240\n"
    "v_fma_f32 v172, v172, s73, v217\n"
    "s_waitcnt lgkmcnt(5)\n"
    "v_mfma_f32_16x16x32_bf16 v[64:67], v[16:19], a[144:147], 0\n"
    "ds_read_b128 v[60:63], v1 offset:10240\n"
    "v_fma_f32 v173, v173, s73, v217\n"
    "v_mfma_f32_16x16x32_bf16 v[64:67], v[20:23], a[148:151], v[64:67]\n"
    "v_exp_f32 v163, v163\n"
    "v_mfma_f32_16x16x32_bf16 v[64:67], v[24:27], a[152:155], v[64:67]\n"
    "v_cvt_pk_bf16_f32 v98, v72, v73\n"
    "v_cvt_pk_bf16_f32 v99, v74, v75\n"
    "v_mfma_f32_16x16x32_bf16 v[64:67], v[28:31], a[156:159], v[64:67]\n"
    "v_exp_f32 v164, v164\n"
    "v_mfma_f32_16x16x32_bf16 v[64:67], v[32:35], a[160:163], v[64:67]\n"
    "v_exp_f32 v165, v165\n"
    "v_mfma_f32_16x16x32_bf16 v[64:67], v[36:39], a[164:167], v[64:67]\n"
    "v_exp_f32 v166, v166\n"
    "v_mfma_f32_16x16x32_bf16 v[68:71], v[16:19], a[168:171], 0\n"
    "v_exp_f32 v167, v167\n"
    "v_mfma_f32_16x16x32_bf16 v[68:71], v[20:23], a[172:175], v[68:71]\n"
    "v_cvt_pk_bf16_f32 v157, v162, v163\n"
    "v_cvt_pk_bf16_f32 v158, v164, v165\n"
    "v_mfma_f32_16x16x32_bf16 v[68:71], v[24:27], a[176:179], v[68:71]\n"
    "v_cvt_pk_bf16_f32 v159, v166, v167\n"
    "v_cvt_pk_bf16_f32 v76, v64, v65\n"
    "v_mfma_f32_16x16x32_bf16 v[68:71], v[28:31], a[180:183], v[68:71]\n"
    "v_cvt_pk_bf16_f32 v77, v66, v67\n"
    "v_fma_f32 v174, v174, s73, v217\n"
    "v_mfma_f32_16x16x32_bf16 v[68:71], v[32:35], a[184:187], v[68:71]\n"
    "v_exp_f32 v168, v168\n"
    "v_mfma_f32_16x16x32_bf16 v[68:71], v[36:39], a[188:191], v[68:71]\n"
    "v_exp_f32 v169, v169\n"
    "v_mfma_f32_16x16x32_bf16 v[72:75], v[16:19], a[192:195], 0\n"
    "ds_read_b128 v[16:19], v2 offset:0\n"
    "v_fma_f32 v175, v175, s73, v217\n"
    "v_mfma_f32_16x16x32_bf16 v[72:75], v[20:23], a[196:199], v[72:75]\n"
    "ds_read_b128 v[20:23], v2 offset:1024\n"
    "v_cvt_pk_bf16_f32 v168, v168, v169\n"
    "v_mfma_f32_16x16x32_bf16 v[72:75], v[24:27], a[200:203], v[72:75]\n"
    "v_cvt_pk_bf16_f32 v124, v68, v69\n"
    "v_cvt_pk_bf16_f32 v125, v70, v71\n"
    "v_mfma_f32_16x16x32_bf16 v[72:75], v[28:31], a[204:207], v[72:75]\n"
    "ds_read_b128 v[24:27], v2 offset:2048\n"
    "v_fma_f32 v180, v180, s73, v217\n"
    "v_mfma_f32_16x16x32_bf16 v[72:75], v[32:35], a[208:211], v[72:75]\n"
    "ds_read_b128 v[28:31], v2 offset:3072\n"
    "v_fma_f32 v181, v181, s73, v217\n"
    "v_mfma_f32_16x16x32_bf16 v[72:75], v[36:39], a[212:215], v[72:75]\n"
    "ds_read_b128 v[32:35], v2 offset:4096\n"
    "v_fma_f32 v182, v182, s73, v217\n"
    "s_waitcnt lgkmcnt(5)\n"
    "v_mfma_f32_16x16x32_bf16 v[64:67], v[40:43], a[144:147], 0\n"
    "ds_read_b128 v[36:39], v2 offset:5120\n"
    "v_fma_f32 v183, v183, s73, v217\n"
    "v_mfma_f32_16x16x32_bf16 v[64:67], v[44:47], a[148:151], v[64:67]\n"
    "v_exp_f32 v170, v170\n"
    "v_mfma_f32_16x16x32_bf16 v[64:67], v[48:51], a[152:155], v[64:67]\n"
    "v_cvt_pk_bf16_f32 v92, v72, v73\n"
    "v_cvt_pk_bf16_f32 v93, v74, v75\n"
    "v_mfma_f32_16x16x32_bf16 v[64:67], v[52:55], a[156:159], v[64:67]\n"
    "v_exp_f32 v171, v171\n"
    "v_mfma_f32_16x16x32_bf16 v[64:67], v[56:59], a[160:163], v[64:67]\n"
    "v_exp_f32 v172, v172\n"
    "v_mfma_f32_16x16x32_bf16 v[64:67], v[60:63], a[164:167], v[64:67]\n"
    "v_exp_f32 v173, v173\n"
    "v_mfma_f32_16x16x32_bf16 v[68:71], v[40:43], a[168:171], 0\n"
    "v_exp_f32 v174, v174\n"
    "v_mfma_f32_16x16x32_bf16 v[68:71], v[44:47], a[172:175], v[68:71]\n"
    "v_exp_f32 v175, v175\n"
    "v_mfma_f32_16x16x32_bf16 v[68:71], v[48:51], a[176:179], v[68:71]\n"
    "v_cvt_pk_bf16_f32 v169, v170, v171\n"
    "v_cvt_pk_bf16_f32 v170, v172, v173\n"
    "v_mfma_f32_16x16x32_bf16 v[68:71], v[52:55], a[180:183], v[68:71]\n"
    "v_cvt_pk_bf16_f32 v78, v64, v65\n"
    "v_cvt_pk_bf16_f32 v79, v66, v67\n"
    "v_mfma_f32_16x16x32_bf16 v[68:71], v[56:59], a[184:187], v[68:71]\n"
    "v_cvt_pk_bf16_f32 v171, v174, v175\n"
    "v_fma_f32 v176, v176, s73, v217\n"
    "v_mfma_f32_16x16x32_bf16 v[68:71], v[60:63], a[188:191], v[68:71]\n"
    "v_exp_f32 v180, v180\n"
    "v_mfma_f32_16x16x32_bf16 v[72:75], v[40:43], a[192:195], 0\n"
    "ds_read_b128 v[40:43], v2 offset:6144\n"
    "v_fma_f32 v177, v177, s73, v217\n"
    "v_mfma_f32_16x16x32_bf16 v[72:75], v[44:47], a[196:199], v[72:75]\n"
    "ds_read_b128 v[44:47], v2 offset:7168\n"
    "v_fma_f32 v178, v178, s73, v217\n"
    "v_mfma_f32_16x16x32_bf16 v[72:75], v[48:51], a[200:203], v[72:75]\n"
    "v_cvt_pk_bf16_f32 v126, v68, v69\n"
    "v_cvt_pk_bf16_f32 v127, v70, v71\n"
    "v_mfma_f32_16x16x32_bf16 v[72:75], v[52:55], a[204:207], v[72:75]\n"
    "ds_read_b128 v[48:51], v2 offset:8192\n"
    "v_fma_f32 v179, v179, s73, v217\n"
    "v_mfma_f32_16x16x32_bf16 v[72:75], v[56:59], a[208:211], v[72:75]\n"
    "ds_read_b128 v[52:55], v2 offset:9216\n"
    "v_max3_f32 v219, v219, v209, v210\n"
    "v_mfma_f32_16x16x32_bf16 v[72:75], v[60:63], a[212:215], v[72:75]\n"
    "ds_read_b128 v[56:59], v2 offset:10240\n"
    "v_max3_f32 v218, v218, v195, v196\n"
    "s_waitcnt lgkmcnt(5)\n"
    "v_mfma_f32_16x16x32_bf16 v[64:67], a[216:219], v[16:19], 0\n"
    "ds_read_b128 v[60:63], v2 offset:11264\n"
    "v_max3_f32 v219, v219, v211, v212\n"
    "v_mfma_f32_16x16x32_bf16 v[64:67], a[220:223], v[20:23], v[64:67]\n"
    "v_exp_f32 v181, v181\n"
    "v_mfma_f32_16x16x32_bf16 v[64:67], a[224:227], v[24:27], v[64:67]\n"
    "v_cvt_pk_bf16_f32 v94, v72, v73\n"
    "v_cvt_pk_bf16_f32 v95, v74, v75\n"
    "v_mfma_f32_16x16x32_bf16 v[64:67], a[228:231], v[28:31], v[64:67]\n"
    "v_exp_f32 v182, v182\n"
    "v_mfma_f32_16x16x32_bf16 v[64:67], a[232:235], v[32:35], v[64:67]\n"
    "v_exp_f32 v183, v183\n"
    "v_mfma_f32_16x16x32_bf16 v[64:67], a[236:239], v[36:39], v[64:67]\n"
    "v_cvt_pk_bf16_f32 v174, v180, v181\n"
    "v_cvt_pk_bf16_f32 v175, v182, v183\n"
    "v_mfma_f32_16x16x32_bf16 v[68:71], a[240:243], v[16:19], 0\n"
    "v_exp_f32 v176, v176\n"
    "v_mfma_f32_16x16x32_bf16 v[68:71], a[244:247], v[20:23], v[68:71]\n"
    "v_exp_f32 v177, v177\n"
    "v_mfma_f32_16x16x32_bf16 v[68:71], a[248:251], v[24:27], v[68:71]\n"
    "v_exp_f32 v178, v178\n"
    "v_mfma_f32_16x16x32_bf16 v[68:71], a[252:255], v[28:31], v[68:71]\n"
    "v_cvt_pk_bf16_f32 v108, v64, v65\n"
    "v_cvt_pk_bf16_f32 v109, v66, v67\n"
    "v_mfma_f32_16x16x32_bf16 v[68:71], v[224:227], v[32:35], v[68:71]\n"
    "v_exp_f32 v179, v179\n"
    "v_mfma_f32_16x16x32_bf16 v[68:71], v[228:231], v[36:39], v[68:71]\n"
    "v_cvt_pk_bf16_f32 v172, v176, v177\n"
    "v_cvt_pk_bf16_f32 v173, v178, v179\n"
    "v_mfma_f32_16x16x32_bf16 v[72:75], v[232:235], v[16:19], 0\n"
    "ds_read_b128 v[16:19], v2 offset:12288\n"
    "v_max3_f32 v218, v218, v197, v198\n"
    "v_mfma_f32_16x16x32_bf16 v[72:75], v[236:239], v[20:23], v[72:75]\n"
    "ds_read_b128 v[20:23], v2 offset:13312\n"
    "v_max3_f32 v219, v219, v213, v214\n"
    "v_mfma_f32_16x16x32_bf16 v[72:75], v[240:243], v[24:27], v[72:75]\n"
    "v_cvt_pk_bf16_f32 v110, v68, v69\n"
    "v_cvt_pk_bf16_f32 v111, v70, v71\n"
    "v_mfma_f32_16x16x32_bf16 v[72:75], v[244:247], v[28:31], v[72:75]\n"
    "buffer_store_dwordx4 v[108:111], v10, s[20:23], s44 offen nt\n"
    "v_mfma_f32_16x16x32_bf16 v[72:75], v[248:251], v[32:35], v[72:75]\n"
    "ds_read_b128 v[24:27], v2 offset:14336\n"
    "v_max_f32 v218, v218, v199\n"
    "v_mfma_f32_16x16x32_bf16 v[72:75], v[252:255], v[36:39], v[72:75]\n"
    "ds_read_b128 v[28:31], v2 offset:15360\n"
    "v_max_f32 v219, v219, v215\n"
    "s_waitcnt lgkmcnt(4)\n"
    "v_mfma_f32_16x16x32_bf16 v[64:67], a[216:219], v[40:43], 0\n"
    "ds_read_b128 v[32:35], v2 offset:16384\n"
    "v_permlane32_swap_b32 v218, v219\n"
    "v_mfma_f32_16x16x32_bf16 v[64:67], a[220:223], v[44:47], v[64:67]\n"
    "ds_read_b128 v[36:39], v2 offset:17408\n"
    "v_max_f32 v218, v218, v219\n"
    "v_mfma_f32_16x16x32_bf16 v[64:67], a[224:227], v[48:51], v[64:67]\n"
    "v_cvt_pk_bf16_f32 v112, v72, v73\n"
    "v_cvt_pk_bf16_f32 v113, v74, v75\n"
    "v_mfma_f32_16x16x32_bf16 v[64:67], a[228:231], v[52:55], v[64:67]\n"
    "buffer_store_dwordx2 v[112:113], v11, s[20:23], s44 offen nt\n"
    "v_mfma_f32_16x16x32_bf16 v[64:67], a[232:235], v[56:59], v[64:67]\n"
    "v_mov_b32 v219, v218\n"
    "v_xor_b32 v0, 32768, v0\n"
    "v_mfma_f32_16x16x32_bf16 v[64:67], a[236:239], v[60:63], v[64:67]\n"
    "v_permlane16_swap_b32 v218, v219\n"
    "v_max_f32 v218, v218, v219\n"
    "v_mfma_f32_16x16x32_bf16 v[68:71], a[240:243], v[40:43], 0\n"
    "v_mul_f32 v218, s74, v218\n"
    "v_mov_b32 v219, v218\n"
    "v_mfma_f32_16x16x32_bf16 v[68:71], a[244:247], v[44:47], v[68:71]\n"
    "v_xor_b32 v1, 32768, v1\n"
    "v_permlane32_swap_b32 v218, v219\n"
    "v_mfma_f32_16x16x32_bf16 v[68:71], a[248:251], v[48:51], v[68:71]\n"
    "v_cvt_pk_bf16_f32 v100, v64, v65\n"
    "v_cvt_pk_bf16_f32 v101, v66, v67\n"
    "v_mfma_f32_16x16x32_bf16 v[68:71], a[252:255], v[52:55], v[68:71]\n"
    "v_fma_f32 v184, v184, s73, v218\n"
    "v_fma_f32 v185, v185, s73, v218\n"
    "v_mfma_f32_16x16x32_bf16 v[68:71], v[224:227], v[56:59], v[68:71]\n"
    "v_exp_f32 v184, v184\n"
    "v_mfma_f32_16x16x32_bf16 v[68:71], v[228:231], v[60:63], v[68:71]\n"
    "v_exp_f32 v185, v185\n"
    "v_mfma_f32_16x16x32_bf16 v[72:75], v[232:235], v[40:43], 0\n"
    "ds_read_b128 v[40:43], v2 offset:18432\n"
    "v_fma_f32 v186, v186, s73, v218\n"
    "v_mfma_f32_16x16x32_bf16 v[72:75], v[236:239], v[44:47], v[72:75]\n"
    "ds_read_b128 v[44:47], v2 offset:19456\n"
    "v_fma_f32 v187, v187, s73, v218\n"
    "v_mfma_f32_16x16x32_bf16 v[72:75], v[240:243], v[48:51], v[72:75]\n"
    "v_cvt_pk_bf16_f32 v102, v68, v69\n"
    "v_cvt_pk_bf16_f32 v103, v70, v71\n"
    "v_mfma_f32_16x16x32_bf16 v[72:75], v[244:247], v[52:55], v[72:75]\n"
    "buffer_store_dwordx4 v[100:103], v10, s[20:23], s45 offen nt\n"
    "v_mfma_f32_16x16x32_bf16 v[72:75], v[248:251], v[56:59], v[72:75]\n"
    "ds_read_b128 v[48:51], v2 offset:20480\n"
    "v_fma_f32 v188, v188, s73, v218\n"
    "v_mfma_f32_16x16x32_bf16 v[72:75], v[252:255], v[60:63], v[72:75]\n"
    "ds_read_b128 v[52:55], v2 offset:21504\n"
    "v_fma_f32 v189, v189, s73, v218\n"
    "s_waitcnt lgkmcnt(4)\n"
    "v_mfma_f32_16x16x32_bf16 v[64:67], a[216:219], v[16:19], 0\n"
    "ds_read_b128 v[56:59], v2 offset:22528\n"
    "v_fma_f32 v190, v190, s73, v218\n"
    "v_mfma_f32_16x16x32_bf16 v[64:67], a[220:223], v[20:23], v[64:67]\n"
    "ds_read_b128 v[60:63], v2 offset:23552\n"
    "v_fma_f32 v191, v191, s73, v218\n"
    "v_mfma_f32_16x16x32_bf16 v[64:67], a[224:227], v[24:27], v[64:67]\n"
    "v_cvt_pk_bf16_f32 v104, v72, v73\n"
    "v_cvt_pk_bf16_f32 v105, v74, v75\n"
    "v_mfma_f32_16x16x32_bf16 v[64:67], a[228:231], v[28:31], v[64:67]\n"
    "buffer_store_dwordx2 v[104:105], v11, s[20:23], s45 offen nt\n"
    "v_mfma_f32_16x16x32_bf16 v[64:67], a[232:235], v[32:35], v[64:67]\n"
    "v_exp_f32 v186, v186\n"
    "v_mfma_f32_16x16x32_bf16 v[64:67], a[236:239], v[36:39], v[64:67]\n"
    "v_exp_f32 v187, v187\n"
    "v_mfma_f32_16x16x32_bf16 v[68:71], a[240:243], v[16:19], 0\n"
    "v_cvt_pk_bf16_f32 v184, v184, v185\n"
    "v_cvt_pk_bf16_f32 v185, v186, v187\n"
    "v_mfma_f32_16x16x32_bf16 v[68:71], a[244:247], v[20:23], v[68:71]\n"
    "v_exp_f32 v188, v188\n"
    "v_mfma_f32_16x16x32_bf16 v[68:71], a[248:251], v[24:27], v[68:71]\n"
    "v_exp_f32 v189, v189\n"
    "v_mfma_f32_16x16x32_bf16 v[68:71], a[252:255], v[28:31], v[68:71]\n"
    "v_cvt_pk_bf16_f32 v108, v64, v65\n"
    "v_cvt_pk_bf16_f32 v109, v66, v67\n"
    "v_mfma_f32_16x16x32_bf16 v[68:71], v[224:227], v[32:35], v[68:71]\n"
    "v_exp_f32 v190, v190\n"
    "v_mfma_f32_16x16x32_bf16 v[68:71], v[228:231], v[36:39], v[68:71]\n"
    "v_exp_f32 v191, v191\n"
    "v_mfma_f32_16x16x32_bf16 v[72:75], v[232:235], v[16:19], 0\n"
    "v_cvt_pk_bf16_f32 v186, v188, v189\n"
    "v_cvt_pk_bf16_f32 v187, v190, v191\n"
    "v_mfma_f32_16x16x32_bf16 v[72:75], v[236:239], v[20:23], v[72:75]\n"
    "v_fma_f32 v192, v192, s73, v218\n"
    "v_fma_f32 v193, v193, s73, v218\n"
    "v_mfma_f32_16x16x32_bf16 v[72:75], v[240:243], v[24:27], v[72:75]\n"
    "v_cvt_pk_bf16_f32 v110, v68, v69\n"
    "v_cvt_pk_bf16_f32 v111, v70, v71\n"
    "v_mfma_f32_16x16x32_bf16 v[72:75], v[244:247], v[28:31], v[72:75]\n"
    "buffer_store_dwordx4 v[108:111], v10, s[20:23], s46 offen nt\n"
    "v_mfma_f32_16x16x32_bf16 v[72:75], v[248:251], v[32:35], v[72:75]\n"
    "v_fma_f32 v194, v194, s73, v218\n"
    "v_fma_f32 v195, v195, s73, v218\n"
    "v_mfma_f32_16x16x32_bf16 v[72:75], v[252:255], v[36:39], v[72:75]\n"
    "v_fma_f32 v196, v196, s73, v218\n"
    "v_fma_f32 v197, v197, s73, v218\n"
    "s_waitcnt lgkmcnt(0)\n"
    "v_mfma_f32_16x16x32_bf16 v[64:67], a[216:219], v[40:43], 0\n"
    "v_fma_f32 v198, v198, s73, v218\n"
    "v_fma_f32 v199, v199, s73, v218\n"
    "v_mfma_f32_16x16x32_bf16 v[64:67], a[220:223], v[44:47], v[64:67]\n"
    "v_exp_f32 v192, v192\n"
    "v_mfma_f32_16x16x32_bf16 v[64:67], a[224:227], v[48:51], v[64:67]\n"
    "v_cvt_pk_bf16_f32 v112, v72, v73\n"
    "v_cvt_pk_bf16_f32 v113, v74, v75\n"
    "v_mfma_f32_16x16x32_bf16 v[64:67], a[228:231], v[52:55], v[64:67]\n"
    "buffer_store_dwordx2 v[112:113], v11, s[20:23], s46 offen nt\n"
    "v_mfma_f32_16x16x32_bf16 v[64:67], a[232:235], v[56:59], v[64:67]\n"
    "v_exp_f32 v193, v193\n"
    "v_mfma_f32_16x16x32_bf16 v[64:67], a[236:239], v[60:63], v[64:67]\n"
    "v_exp_f32 v194, v194\n"
    "v_mfma_f32_16x16x32_bf16 v[68:71], a[240:243], v[40:43], 0\n"
    "v_exp_f32 v195, v195\n"
    "v_mfma_f32_16x16x32_bf16 v[68:71], a[244:247], v[44:47], v[68:71]\n"
    "v_exp_f32 v196, v196\n"
    "v_mfma_f32_16x16x32_bf16 v[68:71], a[248:251], v[48:51], v[68:71]\n"
    "v_exp_f32 v197, v197\n"
    "v_mfma_f32_16x16x32_bf16 v[68:71], a[252:255], v[52:55], v[68:71]\n"
    "v_exp_f32 v198, v198\n"
    "v_mfma_f32_16x16x32_bf16 v[68:71], v[224:227], v[56:59], v[68:71]\n"
    "v_exp_f32 v199, v199\n"
    "v_mfma_f32_16x16x32_bf16 v[68:71], v[228:231], v[60:63], v[68:71]\n"
    "v_cvt_pk_bf16_f32 v188, v192, v193\n"
    "v_cvt_pk_bf16_f32 v189, v194, v195\n"
    "v_mfma_f32_16x16x32_bf16 v[72:75], v[232:235], v[40:43], 0\n"
    "v_cvt_pk_bf16_f32 v190, v196, v197\n"
    "v_cvt_pk_bf16_f32 v191, v198, v199\n"
    "v_mfma_f32_16x16x32_bf16 v[72:75], v[236:239], v[44:47], v[72:75]\n"
    "s_waitcnt vmcnt(6)\n"
    "s_barrier\n"
    "v_mfma_f32_16x16x32_bf16 v[72:75], v[240:243], v[48:51], v[72:75]\n"
    "v_fma_f32 v200, v200, s73, v219\n"
    "v_fma_f32 v201, v201, s73, v219\n"
    "v_mfma_f32_16x16x32_bf16 v[72:75], v[244:247], v[52:55], v[72:75]\n"
    "v_exp_f32 v200, v200\n"
    "v_mfma_f32_16x16x32_bf16 v[72:75], v[248:251], v[56:59], v[72:75]\n"
    "v_exp_f32 v201, v201\n"
    "v_mfma_f32_16x16x32_bf16 v[72:75], v[252:255], v[60:63], v[72:75]\n"
    "v_fma_f32 v202, v202, s73, v219\n"
    "v_fma_f32 v203, v203, s73, v219\n"
    "v_mfma_f32_16x16x32_bf16 v[52:55], v[12:15], v[152:155], 0\n"
    "v_exp_f32 v202, v202\n"
    "v_mfma_f32_16x16x32_bf16 v[52:55], v[12:15], v[156:159], v[52:55]\n"
    "v_exp_f32 v203, v203\n"
    "v_mfma_f32_16x16x32_bf16 v[40:43], v[76:79], v[152:155], 0\n"
    "v_fma_f32 v204, v204, s73, v219\n"
    "v_fma_f32 v205, v205, s73, v219\n"
    "v_mfma_f32_16x16x32_bf16 v[40:43], v[80:83], v[156:159], v[40:43]\n"
    "v_fma_f32 v206, v206, s73, v219\n"
    "v_fma_f32 v207, v207, s73, v219\n"
    "v_mfma_f32_16x16x32_bf16 v[44:47], v[124:127], v[152:155], 0\n"
    "ds_read_b128 v[16:19], v0 offset:0\n"
    "v_cvt_pk_bf16_f32 v200, v200, v201\n"
    "v_mfma_f32_16x16x32_bf16 v[44:47], v[128:131], v[156:159], v[44:47]\n"
    "ds_read_b128 v[20:23], v1 offset:0\n"
    "v_cvt_pk_bf16_f32 v201, v202, v203\n"
    "v_mfma_f32_16x16x32_bf16 v[48:51], v[92:95], v[152:155], 0\n"
    "ds_read_b128 v[24:27], v0 offset:2048\n"
    "v_fma_f32 v212, v212, s73, v219\n"
    "v_mfma_f32_16x16x32_bf16 v[48:51], v[96:99], v[156:159], v[48:51]\n"
    "ds_read_b128 v[28:31], v1 offset:2048\n"
    "v_fma_f32 v213, v213, s73, v219\n"
    "v_mfma_f32_16x16x32_bf16 v[180:183], v[12:15], v[168:171], 0\n"
    "ds_read_b128 v[32:35], v0 offset:4096\n"
    "v_fma_f32 v214, v214, s73, v219\n"
    "v_mfma_f32_16x16x32_bf16 v[180:183], v[12:15], v[172:175], v[180:183]\n"
    "ds_read_b128 v[36:39], v1 offset:4096\n"
    "v_fma_f32 v215, v215, s73, v219\n"
    "v_mfma_f32_16x16x32_bf16 v[160:163], v[76:79], v[168:171], 0\n"
    "v_exp_f32 v204, v204\n"
    "v_mfma_f32_16x16x32_bf16 v[160:163], v[80:83], v[172:175], v[160:163]\n"
    "v_exp_f32 v205, v205\n"
    "v_mfma_f32_16x16x32_bf16 v[164:167], v[124:127], v[168:171], 0\n"
    "v_exp_f32 v206, v206\n"
    "v_mfma_f32_16x16x32_bf16 v[164:167], v[128:131], v[172:175], v[164:167]\n"
    "v_exp_f32 v207, v207\n"
    "v_mfma_f32_16x16x32_bf16 v[176:179], v[92:95], v[168:171], 0\n"
    "v_cvt_pk_bf16_f32 v202, v204, v205\n"
    "v_cvt_pk_bf16_f32 v203, v206, v207\n"
    "v_mfma_f32_16x16x32_bf16 v[176:179], v[96:99], v[172:175], v[176:179]\n"
    "v_exp_f32 v212, v212\n"
    "v_mfma_f32_16x16x32_bf16 v[136:139], v[12:15], v[184:187], 0\n"
    "v_exp_f32 v213, v213\n"
    "v_mfma_f32_16x16x32_bf16 v[136:139], v[12:15], v[188:191], v[136:139]\n"
    "v_exp_f32 v214, v214\n"
    "v_mfma_f32_16x16x32_bf16 v[84:87], v[76:79], v[184:187], 0\n"
    "v_exp_f32 v215, v215\n"
    "v_mfma_f32_16x16x32_bf16 v[84:87], v[80:83], v[188:191], v[84:87]\n"
    "v_cvt_pk_bf16_f32 v206, v212, v213\n"
    "v_cvt_pk_bf16_f32 v207, v214, v215\n"
    "v_mfma_f32_16x16x32_bf16 v[88:91], v[124:127], v[184:187], 0\n"
    "v_fma_f32 v208, v208, s73, v219\n"
    "v_fma_f32 v209, v209, s73, v219\n"
    "v_mfma_f32_16x16x32_bf16 v[88:91], v[128:131], v[188:191], v[88:91]\n"
    "v_fma_f32 v210, v210, s73, v219\n"
    "v_fma_f32 v211, v211, s73, v219\n"
    "v_mfma_f32_16x16x32_bf16 v[132:135], v[92:95], v[184:187], 0\n"
    "v_exp_f32 v208, v208\n"
    "v_mfma_f32_16x16x32_bf16 v[132:135], v[96:99], v[188:191], v[132:135]\n"
    "v_exp_f32 v209, v209\n"
    "v_mfma_f32_16x16x32_bf16 v[212:215], v[12:15], v[200:203], 0\n"
    "v_exp_f32 v210, v210\n"
    "v_exp_f32 v211, v211\n"
    "v_cvt_pk_bf16_f32 v204, v208, v209\n"
    "v_cvt_pk_bf16_f32 v205, v210, v211\n"
    "v_cvt_pk_bf16_f32 v100, v64, v65\n"
    "v_cvt_pk_bf16_f32 v101, v66, v67\n"
    "v_mfma_f32_16x16x32_bf16 v[212:215], v[12:15], v[204:207], v[212:215]\n"
    "v_cvt_pk_bf16_f32 v102, v68, v69\n"
    "v_cvt_pk_bf16_f32 v103, v70, v71\n"
    "v_mfma_f32_16x16x32_bf16 v[192:195], v[76:79], v[200:203], 0\n"
    "buffer_store_dwordx4 v[100:103], v10, s[20:23], s47 offen nt\n"
    "v_mfma_f32_16x16x32_bf16 v[192:195], v[80:83], v[204:207], v[192:195]\n"
    "v_cvt_pk_bf16_f32 v104, v72, v73\n"
    "v_cvt_pk_bf16_f32 v105, v74, v75\n"
    "v_mfma_f32_16x16x32_bf16 v[196:199], v[124:127], v[200:203], 0\n"
    "s_mov_b64 s[44:45], s[48:49]\n"
    "s_mov_b64 s[48:49], s[52:53]\n"
    "v_mfma_f32_16x16x32_bf16 v[196:199], v[128:131], v[204:207], v[196:199]\n"
    "buffer_store_dwordx2 v[104:105], v11, s[20:23], s47 offen nt\n"
    "v_mfma_f32_16x16x32_bf16 v[208:211], v[92:95], v[200:203], 0\n"
    "s_mov_b64 s[46:47], s[50:51]\n"
    "s_mov_b64 s[50:51], s[54:55]\n"
    "v_mfma_f32_16x16x32_bf16 v[208:211], v[96:99], v[204:207], v[208:211]\n"
    "s_mov_b64 s[52:53], s[56:57]\n"
    "s_mov_b64 s[54:55], s[58:59]\n"
    "s_sub_u32 s76, s76, 1\n"
    "s_cmp_lg_u32 s76, 0\n"
    "s_cbranch_scc1 .Lswin_loop%=\n"
    "s_cmp_lt_u32 s14, 64\n"
    "s_cbranch_scc1 .Lswin_left%=\n"
    "v_rcp_f32 v220, v52\n"
    "v_rcp_f32 v221, v180\n"
    "v_mul_f32 v40, v220, v40\n"
    "v_mul_f32 v41, v220, v41\n"
    "v_mul_f32 v42, v220, v42\n"
    "v_mul_f32 v43, v220, v43\n"
    "v_mul_f32 v44, v220, v44\n"
    "v_mul_f32 v45, v220, v45\n"
    "v_mul_f32 v46, v220, v46\n"
    "v_mul_f32 v47, v220, v47\n"
    "v_mul_f32 v48, v220, v48\n"
    "v_mul_f32 v49, v220, v49\n"
    "v_mul_f32 v50, v220, v50\n"
    "v_mul_f32 v51, v220, v51\n"
    "v_mul_f32 v160, v221, v160\n"
    "v_mul_f32 v161, v221, v161\n"
    "v_mul_f32 v162, v221, v162\n"
    "v_mul_f32 v163, v221, v163\n"
    "v_mul_f32 v164, v221, v164\n"
    "v_mul_f32 v165, v221, v165\n"
    "v_mul_f32 v166, v221, v166\n"
    "v_mul_f32 v167, v221, v167\n"
    "v_mul_f32 v176, v221, v176\n"
    "v_mul_f32 v177, v221, v177\n"
    "v_mul_f32 v178, v221, v178\n"
    "v_mul_f32 v179, v221, v179\n"
    "v_cvt_pk_bf16_f32 v116, v40, v41\n"
    "v_cvt_pk_bf16_f32 v117, v42, v43\n"
    "v_cvt_pk_bf16_f32 v118, v44, v45\n"
    "v_cvt_pk_bf16_f32 v119, v46, v47\n"
    "v_cvt_pk_bf16_f32 v120, v48, v49\n"
    "v_cvt_pk_bf16_f32 v121, v50, v51\n"
    "v_cvt_pk_bf16_f32 v108, v160, v161\n"
    "v_cvt_pk_bf16_f32 v109, v162, v163\n"
    "v_cvt_pk_bf16_f32 v110, v164, v165\n"
    "v_cvt_pk_bf16_f32 v111, v166, v167\n"
    "v_cvt_pk_bf16_f32 v112, v176, v177\n"
    "v_cvt_pk_bf16_f32 v113, v178, v179\n"
    "ds_write_b128 v4, v[116:119] offset:0\n"
    "ds_write_b64 v5, v[120:121] offset:0\n"
    "v_rcp_f32 v220, v136\n"
    "ds_write_b128 v4, v[108:111] offset:6144\n"
    "ds_write_b64 v5, v[112:113] offset:6144\n"
    "v_rcp_f32 v221, v212\n"
    "v_mul_f32 v84, v220, v84\n"
    "v_mul_f32 v85, v220, v85\n"
    "v_mul_f32 v86, v220, v86\n"
    "v_mul_f32 v87, v220, v87\n"
    "v_mul_f32 v88, v220, v88\n"
    "v_mul_f32 v89, v220, v89\n"
    "v_mul_f32 v90, v220, v90\n"
    "v_mul_f32 v91, v220, v91\n"
    "v_mul_f32 v132, v220, v132\n"
    "v_mul_f32 v133, v220, v133\n"
    "v_mul_f32 v134, v220, v134\n"
    "v_mul_f32 v135, v220, v135\n"
    "v_mul_f32 v192, v221, v192\n"
    "v_mul_f32 v193, v221, v193\n"
    "v_mul_f32 v194, v221, v194\n"
    "v_mul_f32 v195, v221, v195\n"
    "v_mul_f32 v196, v221, v196\n"
    "v_mul_f32 v197, v221, v197\n"
    "v_mul_f32 v198, v221, v198\n"
    "v_mul_f32 v199, v221, v199\n"
    "v_mul_f32 v208, v221, v208\n"
    "v_mul_f32 v209, v221, v209\n"
    "v_mul_f32 v210, v221, v210\n"
    "v_mul_f32 v211, v221, v211\n"
    "s_waitcnt lgkmcnt(0)\n"
    "s_barrier\n"
    "ds_read_b128 v[16:19], v2 offset:0\n"
    "v_cvt_pk_bf16_f32 v116, v84, v85\n"
    "ds_read_b128 v[20:23], v2 offset:1024\n"
    "v_cvt_pk_bf16_f32 v117, v86, v87\n"
    "ds_read_b128 v[24:27], v2 offset:2048\n"
    "v_cvt_pk_bf16_f32 v118, v88, v89\n"
    "ds_read_b128 v[28:31], v2 offset:3072\n"
    "v_cvt_pk_bf16_f32 v119, v90, v91\n"
    "ds_read_b128 v[32:35], v2 offset:4096\n"
    "v_cvt_pk_bf16_f32 v120, v132, v133\n"
    "ds_read_b128 v[36:39], v2 offset:5120\n"
    "v_cvt_pk_bf16_f32 v121, v134, v135\n"
    "v_cvt_pk_bf16_f32 v108, v192, v193\n"
    "v_cvt_pk_bf16_f32 v109, v194, v195\n"
    "v_cvt_pk_bf16_f32 v110, v196, v197\n"
    "v_cvt_pk_bf16_f32 v111, v198, v199\n"
    "v_cvt_pk_bf16_f32 v112, v208, v209\n"
    "v_cvt_pk_bf16_f32 v113, v210, v211\n"
    "ds_write_b128 v4, v[116:119] offset:12288\n"
    "ds_write_b64 v5, v[120:121] offset:12288\n"
    "s_waitcnt lgkmcnt(7)\n"
    "v_mfma_f32_16x16x32_bf16 v[64:67], a[216:219], v[16:19], 0\n"
    "ds_write_b128 v4, v[108:111] offset:18432\n"
    "s_waitcnt lgkmcnt(4)\n"
    "v_mfma_f32_16x16x32_bf16 v[64:67], a[220:223], v[20:23], v[64:67]\n"
    "ds_write_b64 v5, v[112:113] offset:18432\n"
    "v_mfma_f32_16x16x32_bf16 v[64:67], a[224:227], v[24:27], v[64:67]\n"
    "ds_read_b128 v[152:155], v2 offset:6144\n"
    "v_mbcnt_lo_u32_b32 v56, -1, 0\n"
    "v_mfma_f32_16x16x32_bf16 v[64:67], a[228:231], v[28:31], v[64:67]\n"
    "ds_read_b128 v[156:159], v2 offset:7168\n"
    "v_mbcnt_hi_u32_b32 v56, -1, v56\n"
    "v_mfma_f32_16x16x32_bf16 v[64:67], a[232:235], v[32:35], v[64:67]\n"
    "ds_read_b128 v[160:163], v2 offset:8192\n"
    "v_lshrrev_b32 v58, 4, v56\n"
    "s_waitcnt lgkmcnt(5)\n"
    "v_mfma_f32_16x16x32_bf16 v[64:67], a[236:239], v[36:39], v[64:67]\n"
    "ds_read_b128 v[164:167], v2 offset:9216\n"
    "s_mul_i32 s88, s77, 6\n"
    "v_mfma_f32_16x16x32_bf16 v[68:71], a[240:243], v[16:19], 0\n"
    "ds_read_b128 v[168:171], v2 offset:10240\n"
    "v_and_b32 v57, 15, v56\n"
    "v_mfma_f32_16x16x32_bf16 v[68:71], a[244:247], v[20:23], v[68:71]\n"
    "ds_read_b128 v[172:175], v2 offset:11264\n"
    "v_and_b32 v59, 7, v56\n"
    "v_mfma_f32_16x16x32_bf16 v[68:71], a[248:251], v[24:27], v[68:71]\n"
    "v_cvt_pk_bf16_f32 v100, v64, v65\n"
    "v_cvt_pk_bf16_f32 v101, v66, v67\n"
    "v_mfma_f32_16x16x32_bf16 v[68:71], a[252:255], v[28:31], v[68:71]\n"
    "v_add_u32 v60, s88, v58\n"
    "v_xor_b32 v60, v60, v59\n"
    "v_mfma_f32_16x16x32_bf16 v[68:71], v[224:227], v[32:35], v[68:71]\n"
    "v_mul_u32_u24 v57, 384, v57\n"
    "v_lshl_add_u32 v60, v60, 4, v57\n"
    "v_mfma_f32_16x16x32_bf16 v[68:71], v[228:231], v[36:39], v[68:71]\n"
    "v_lshrrev_b32 v61, 1, v58\n"
    "s_add_u32 s88, s88, 4\n"
    "v_mfma_f32_16x16x32_bf16 v[72:75], v[232:235], v[16:19], 0\n"
    "v_add_u32 v61, s88, v61\n"
    "v_xor_b32 v61, v61, v59\n"
    "v_mfma_f32_16x16x32_bf16 v[72:75], v[236:239], v[20:23], v[72:75]\n"
    "v_lshl_add_u32 v61, v61, 4, v57\n"
    "v_and_b32 v62, 1, v58\n"
    "v_mfma_f32_16x16x32_bf16 v[72:75], v[240:243], v[24:27], v[72:75]\n"
    "v_cvt_pk_bf16_f32 v102, v68, v69\n"
    "v_cvt_pk_bf16_f32 v103, v70, v71\n"
    "v_mfma_f32_16x16x32_bf16 v[72:75], v[244:247], v[28:31], v[72:75]\n"
    "ds_write_b128 v60, v[100:103] offset:0\n"
    "v_mfma_f32_16x16x32_bf16 v[72:75], v[248:251], v[32:35], v[72:75]\n"
    "v_lshl_add_u32 v61, v62, 3, v61\n"
    "v_lshrrev_b32 v63, 3, v56\n"
    "v_mfma_f32_16x16x32_bf16 v[72:75], v[252:255], v[36:39], v[72:75]\n"
    "v_xor_b32 v62, v59, v58\n"
    "v_xor_b32 v62, v62, v63\n"
    "v_mul_u32_u24 v63, 384, v63\n"
    "s_mul_i32 s89, s77, 6144\n"
    "v_lshl_add_u32 v62, v62, 4, v63\n"
    "v_add_u32 v62, s89, v62\n"
    "v_xor_b32 v63, 64, v62\n"
    "s_cmp_ge_u32 s77, 2\n"
    "s_cselect_b32 s44, s46, s44\n"
    "s_cselect_b32 s45, s47, s45\n"
    "v_cvt_pk_bf16_f32 v104, v72, v73\n"
    "v_cvt_pk_bf16_f32 v105, v74, v75\n"
    "ds_write_b64 v61, v[104:105] offset:0\n"
    "s_waitcnt lgkmcnt(2)\n"
    "s_barrier\n"
    "v_mfma_f32_16x16x32_bf16 v[64:67], a[216:219], v[152:155], 0\n"
    "ds_read_b128 v[76:79], v2 offset:12288\n"
    "v_mfma_f32_16x16x32_bf16 v[64:67], a[220:223], v[156:159], v[64:67]\n"
    "ds_read_b128 v[80:83], v2 offset:13312\n"
    "v_mfma_f32_16x16x32_bf16 v[64:67], a[224:227], v[160:163], v[64:67]\n"
    "ds_read_b128 v[84:87], v2 offset:14336\n"
    "v_mfma_f32_16x16x32_bf16 v[64:67], a[228:231], v[164:167], v[64:67]\n"
    "ds_read_b128 v[88:91], v2 offset:15360\n"
    "v_mfma_f32_16x16x32_bf16 v[64:67], a[232:235], v[168:171], v[64:67]\n"
    "ds_read_b128 v[92:95], v2 offset:16384\n"
    "v_mfma_f32_16x16x32_bf16 v[64:67], a[236:239], v[172:175], v[64:67]\n"
    "ds_read_b128 v[96:99], v2 offset:17408\n"
    "v_mfma_f32_16x16x32_bf16 v[68:71], a[240:243], v[152:155], 0\n"
    "ds_read_b128 v[184:187], v2 offset:18432\n"
    "v_mfma_f32_16x16x32_bf16 v[68:71], a[244:247], v[156:159], v[68:71]\n"
    "ds_read_b128 v[188:191], v2 offset:19456\n"
    "v_mfma_f32_16x16x32_bf16 v[68:71], a[248:251], v[160:163], v[68:71]\n"
    "ds_read_b128 v[192:195], v2 offset:20480\n"
    "v_mfma_f32_16x16x32_bf16 v[68:71], a[252:255], v[164:167], v[68:71]\n"
    "v_cvt_pk_bf16_f32 v108, v64, v65\n"
    "v_cvt_pk_bf16_f32 v109, v66, v67\n"
    "v_mfma_f32_16x16x32_bf16 v[68:71], v[224:227], v[168:171], v[68:71]\n"
    "ds_read_b128 v[196:199], v2 offset:21504\n"
    "v_mfma_f32_16x16x32_bf16 v[68:71], v[228:231], v[172:175], v[68:71]\n"
    "ds_read_b128 v[200:203], v2 offset:22528\n"
    "v_mfma_f32_16x16x32_bf16 v[72:75], v[232:235], v[152:155], 0\n"
    "ds_read_b128 v[204:207], v2 offset:23552\n"
    "v_mfma_f32_16x16x32_bf16 v[72:75], v[236:239], v[156:159], v[72:75]\n"
    "s_bitcmp1_b32 s77, 0\n"
    "s_cselect_b32 s44, s45, s44\n"
    "v_mfma_f32_16x16x32_bf16 v[72:75], v[240:243], v[160:163], v[72:75]\n"
    "v_mfma_f32_16x16x32_bf16 v[72:75], v[244:247], v[164:167], v[72:75]\n"
    "s_nop 0\n"
    "v_cvt_pk_bf16_f32 v110, v68, v69\n"
    "v_mfma_f32_16x16x32_bf16 v[72:75], v[248:251], v[168:171], v[72:75]\n"
    "v_cvt_pk_bf16_f32 v111, v70, v71\n"
    "v_mfma_f32_16x16x32_bf16 v[72:75], v[252:255], v[172:175], v[72:75]\n"
    "ds_write_b128 v60, v[108:111] offset:6144\n"
    "s_waitcnt lgkmcnt(4)\n"
    "v_mfma_f32_16x16x32_bf16 v[64:67], a[216:219], v[76:79], 0\n"
    "v_mfma_f32_16x16x32_bf16 v[64:67], a[220:223], v[80:83], v[64:67]\n"
    "v_mfma_f32_16x16x32_bf16 v[64:67], a[224:227], v[84:87], v[64:67]\n"
    "v_mfma_f32_16x16x32_bf16 v[64:67], a[228:231], v[88:91], v[64:67]\n"
    "v_mfma_f32_16x16x32_bf16 v[64:67], a[232:235], v[92:95], v[64:67]\n"
    "v_mfma_f32_16x16x32_bf16 v[64:67], a[236:239], v[96:99], v[64:67]\n"
    "s_nop 0\n"
    "v_cvt_pk_bf16_f32 v112, v72, v73\n"
    "v_mfma_f32_16x16x32_bf16 v[68:71], a[240:243], v[76:79], 0\n"
    "v_cvt_pk_bf16_f32 v113, v74, v75\n"
    "v_mfma_f32_16x16x32_bf16 v[68:71], a[244:247], v[80:83], v[68:71]\n"
    "ds_write_b64 v61, v[112:113] offset:6144\n"
    "v_mfma_f32_16x16x32_bf16 v[68:71], a[248:251], v[84:87], v[68:71]\n"
    "s_nop 0\n"
    "v_cvt_pk_bf16_f32 v116, v64, v65\n"
    "v_mfma_f32_16x16x32_bf16 v[68:71], a[252:255], v[88:91], v[68:71]\n"
    "v_cvt_pk_bf16_f32 v117, v66, v67\n"
    "v_mfma_f32_16x16x32_bf16 v[68:71], v[224:227], v[92:95], v[68:71]\n"
    "v_mfma_f32_16x16x32_bf16 v[68:71], v[228:231], v[96:99], v[68:71]\n"
    "v_mfma_f32_16x16x32_bf16 v[72:75], v[232:235], v[76:79], 0\n"
    "v_mfma_f32_16x16x32_bf16 v[72:75], v[236:239], v[80:83], v[72:75]\n"
    "v_mfma_f32_16x16x32_bf16 v[72:75], v[240:243], v[84:87], v[72:75]\n"
    "v_mfma_f32_16x16x32_bf16 v[72:75], v[244:247], v[88:91], v[72:75]\n"
    "v_mfma_f32_16x16x32_bf16 v[72:75], v[248:251], v[92:95], v[72:75]\n"
    "v_mfma_f32_16x16x32_bf16 v[72:75], v[252:255], v[96:99], v[72:75]\n"
    "v_mfma_f32_16x16x32_bf16 v[64:67], a[216:219], v[184:187], 0\n"
    "s_nop 0\n"
    "v_cvt_pk_bf16_f32 v118, v68, v69\n"
    "v_mfma_f32_16x16x32_bf16 v[64:67], a[220:223], v[188:191], v[64:67]\n"
    "v_cvt_pk_bf16_f32 v119, v70, v71\n"
    "v_mfma_f32_16x16x32_bf16 v[64:67], a[224:227], v[192:195], v[64:67]\n"
    "ds_write_b128 v60, v[116:119] offset:12288\n"
    "s_waitcnt lgkmcnt(1)\n"
    "v_mfma_f32_16x16x32_bf16 v[64:67], a[228:231], v[196:199], v[64:67]\n"
    "v_cvt_pk_bf16_f32 v120, v72, v73\n"
    "v_cvt_pk_bf16_f32 v121, v74, v75\n"
    "v_mfma_f32_16x16x32_bf16 v[64:67], a[232:235], v[200:203], v[64:67]\n"
    "ds_write_b64 v61, v[120:121] offset:12288\n"
    "v_mfma_f32_16x16x32_bf16 v[64:67], a[236:239], v[204:207], v[64:67]\n"
    "v_mfma_f32_16x16x32_bf16 v[68:71], a[240:243], v[184:187], 0\n"
    "v_mfma_f32_16x16x32_bf16 v[68:71], a[244:247], v[188:191], v[68:71]\n"
    "v_mfma_f32_16x16x32_bf16 v[68:71], a[248:251], v[192:195], v[68:71]\n"
    "v_mfma_f32_16x16x32_bf16 v[68:71], a[252:255], v[196:199], v[68:71]\n"
    "v_mfma_f32_16x16x32_bf16 v[68:71], v[224:227], v[200:203], v[68:71]\n"
    "v_mfma_f32_16x16x32_bf16 v[68:71], v[228:231], v[204:207], v[68:71]\n"
    "v_mfma_f32_16x16x32_bf16 v[72:75], v[232:235], v[184:187], 0\n"
    "s_nop 0\n"
    "v_cvt_pk_bf16_f32 v124, v64, v65\n"
    "v_mfma_f32_16x16x32_bf16 v[72:75], v[236:239], v[188:191], v[72:75]\n"
    "v_cvt_pk_bf16_f32 v125, v66, v67\n"
    "v_mfma_f32_16x16x32_bf16 v[72:75], v[240:243], v[192:195], v[72:75]\n"
    "v_mfma_f32_16x16x32_bf16 v[72:75], v[244:247], v[196:199], v[72:75]\n"
    "s_nop 0\n"
    "v_cvt_pk_bf16_f32 v126, v68, v69\n"
    "v_mfma_f32_16x16x32_bf16 v[72:75], v[248:251], v[200:203], v[72:75]\n"
    "v_cvt_pk_bf16_f32 v127, v70, v71\n"
    "v_mfma_f32_16x16x32_bf16 v[72:75], v[252:255], v[204:207], v[72:75]\n"
    "ds_write_b128 v60, v[124:127] offset:18432\n"
    "s_nop 0\n"
    "s_nop 0\n"
    "s_nop 0\n"
    "s_nop 0\n"
    "s_nop 0\n"
    "s_nop 0\n"
    "s_nop 0\n"
    "v_cvt_pk_bf16_f32 v128, v72, v73\n"
    "v_cvt_pk_bf16_f32 v129, v74, v75\n"
    "ds_write_b64 v61, v[128:129] offset:18432\n"
    "s_waitcnt lgkmcnt(0)\n"
    "s_barrier\n"
    "ds_read_b128 v[152:155], v62 offset:0\n"
    "ds_read_b128 v[156:159], v63 offset:3072\n"
    "ds_read_b128 v[160:163], v62 offset:128\n"
    "ds_read_b128 v[164:167], v63 offset:3200\n"
    "ds_read_b128 v[168:171], v62 offset:256\n"
    "ds_read_b128 v[172:175], v63 offset:3328\n"
    "s_waitcnt lgkmcnt(5)\n"
    "buffer_store_dwordx4 v[152:155], v6, s[20:23], s44 offen offset:0 nt\n"
    "s_waitcnt lgkmcnt(4)\n"
    "buffer_store_dwordx4 v[156:159], v7, s[20:23], s44 offen offset:0 nt\n"
    "s_waitcnt lgkmcnt(3)\n"
    "buffer_store_dwordx4 v[160:163], v6, s[20:23], s44 offen offset:128 nt\n"
    "s_waitcnt lgkmcnt(2)\n"
    "buffer_store_dwordx4 v[164:167], v7, s[20:23], s44 offen offset:128 nt\n"
    "s_waitcnt lgkmcnt(1)\n"
    "buffer_store_dwordx4 v[168:171], v6, s[20:23], s44 offen offset:256 nt\n"
    "s_waitcnt lgkmcnt(0)\n"
    "buffer_store_dwordx4 v[172:175], v7, s[20:23], s44 offen offset:256 nt\n"
    "s_branch .Lswin_fin%=\n"
    ".Lswin_left%=:\n"
    "s_waitcnt lgkmcnt(0)\n"
    "v_mfma_f32_16x16x32_bf16 v[64:67], a[72:75], v[16:19], 0\n"
    "v_rcp_f32 v220, v52\n"
    "v_mfma_f32_16x16x32_bf16 v[64:67], a[76:79], v[20:23], v[64:67]\n"
    "v_mul_f32 v40, v220, v40\n"
    "v_mul_f32 v41, v220, v41\n"
    "v_mfma_f32_16x16x32_bf16 v[64:67], a[80:83], v[24:27], v[64:67]\n"
    "v_mul_f32 v42, v220, v42\n"
    "v_mul_f32 v43, v220, v43\n"
    "v_mfma_f32_16x16x32_bf16 v[64:67], a[84:87], v[28:31], v[64:67]\n"
    "v_cvt_pk_bf16_f32 v116, v40, v41\n"
    "v_cvt_pk_bf16_f32 v117, v42, v43\n"
    "v_mfma_f32_16x16x32_bf16 v[64:67], a[88:91], v[32:35], v[64:67]\n"
    "ds_read_b128 v[40:43], v0 offset:6144\n"
    "v_mul_f32 v44, v220, v44\n"
    "v_mfma_f32_16x16x32_bf16 v[64:67], a[92:95], v[36:39], v[64:67]\n"
    "v_mul_f32 v45, v220, v45\n"
    "v_mul_f32 v46, v220, v46\n"
    "v_mfma_f32_16x16x32_bf16 v[68:71], a[96:99], v[16:19], 0\n"
    "v_mul_f32 v47, v220, v47\n"
    "v_cvt_pk_bf16_f32 v118, v44, v45\n"
    "v_mfma_f32_16x16x32_bf16 v[68:71], a[100:103], v[20:23], v[68:71]\n"
    "v_cvt_pk_bf16_f32 v119, v46, v47\n"
    "ds_read_b128 v[44:47], v1 offset:6144\n"
    "v_mfma_f32_16x16x32_bf16 v[68:71], a[104:107], v[24:27], v[68:71]\n"
    "v_cvt_pk_bf16_f32 v124, v64, v65\n"
    "v_cvt_pk_bf16_f32 v125, v66, v67\n"
    "v_mfma_f32_16x16x32_bf16 v[68:71], a[108:111], v[28:31], v[68:71]\n"
    "v_mul_f32 v48, v220, v48\n"
    "v_mul_f32 v49, v220, v49\n"
    "v_mfma_f32_16x16x32_bf16 v[68:71], a[112:115], v[32:35], v[68:71]\n"
    "v_mul_f32 v50, v220, v50\n"
    "v_mul_f32 v51, v220, v51\n"
    "v_mfma_f32_16x16x32_bf16 v[68:71], a[116:119], v[36:39], v[68:71]\n"
    "v_cvt_pk_bf16_f32 v120, v48, v49\n"
    "v_cvt_pk_bf16_f32 v121, v50, v51\n"
    "v_mfma_f32_16x16x32_bf16 v[72:75], a[120:123], v[16:19], 0\n"
    "ds_read_b128 v[48:51], v0 offset:8192\n"
    "s_mul_i32 s92, s15, 6144\n"
    "v_mfma_f32_16x16x32_bf16 v[72:75], a[124:127], v[20:23], v[72:75]\n"
    "ds_read_b128 v[52:55], v1 offset:8192\n"
    "v_add_u32 v216, s92, v0\n"
    "v_mfma_f32_16x16x32_bf16 v[72:75], a[128:131], v[24:27], v[72:75]\n"
    "v_cvt_pk_bf16_f32 v126, v68, v69\n"
    "v_cvt_pk_bf16_f32 v127, v70, v71\n"
    "v_mfma_f32_16x16x32_bf16 v[72:75], a[132:135], v[28:31], v[72:75]\n"
    "ds_read_b128 v[56:59], v0 offset:10240\n"
    "v_add_u32 v217, s92, v1\n"
    "v_mfma_f32_16x16x32_bf16 v[72:75], a[136:139], v[32:35], v[72:75]\n"
    "ds_read_b128 v[60:63], v1 offset:10240\n"
    "s_lshl_b32 s93, s15, 12\n"
    "v_mfma_f32_16x16x32_bf16 v[72:75], a[140:143], v[36:39], v[72:75]\n"
    "v_rcp_f32 v221, v180\n"
    "v_mfma_f32_16x16x32_bf16 v[64:67], v[16:19], a[144:147], 0\n"
    "v_mul_f32 v176, v221, v176\n"
    "v_mul_f32 v177, v221, v177\n"
    "v_mfma_f32_16x16x32_bf16 v[64:67], v[20:23], a[148:151], v[64:67]\n"
    "v_cvt_pk_bf16_f32 v112, v176, v177\n"
    "v_mul_f32 v178, v221, v178\n"
    "v_mfma_f32_16x16x32_bf16 v[64:67], v[24:27], a[152:155], v[64:67]\n"
    "v_cvt_pk_bf16_f32 v140, v72, v73\n"
    "v_cvt_pk_bf16_f32 v141, v74, v75\n"
    "v_mfma_f32_16x16x32_bf16 v[64:67], v[28:31], a[156:159], v[64:67]\n"
    "v_mul_f32 v179, v221, v179\n"
    "v_cvt_pk_bf16_f32 v113, v178, v179\n"
    "v_mfma_f32_16x16x32_bf16 v[64:67], v[32:35], a[160:163], v[64:67]\n"
    "ds_write_b64 v5, v[120:121] offset:0\n"
    "v_mfma_f32_16x16x32_bf16 v[64:67], v[36:39], a[164:167], v[64:67]\n"
    "v_rcp_f32 v220, v136\n"
    "v_mfma_f32_16x16x32_bf16 v[68:71], v[16:19], a[168:171], 0\n"
    "v_mul_f32 v132, v220, v132\n"
    "v_mul_f32 v133, v220, v133\n"
    "v_mfma_f32_16x16x32_bf16 v[68:71], v[20:23], a[172:175], v[68:71]\n"
    "v_cvt_pk_bf16_f32 v120, v132, v133\n"
    "v_mul_f32 v134, v220, v134\n"
    "v_mfma_f32_16x16x32_bf16 v[68:71], v[24:27], a[176:179], v[68:71]\n"
    "v_cvt_pk_bf16_f32 v168, v64, v65\n"
    "v_cvt_pk_bf16_f32 v169, v66, v67\n"
    "v_mfma_f32_16x16x32_bf16 v[68:71], v[28:31], a[180:183], v[68:71]\n"
    "v_mul_f32 v135, v220, v135\n"
    "v_cvt_pk_bf16_f32 v121, v134, v135\n"
    "v_mfma_f32_16x16x32_bf16 v[68:71], v[32:35], a[184:187], v[68:71]\n"
    "v_mul_f32 v160, v221, v160\n"
    "v_mul_f32 v161, v221, v161\n"
    "v_mfma_f32_16x16x32_bf16 v[68:71], v[36:39], a[188:191], v[68:71]\n"
    "v_mul_f32 v162, v221, v162\n"
    "v_mul_f32 v163, v221, v163\n"
    "v_mfma_f32_16x16x32_bf16 v[72:75], v[16:19], a[192:195], 0\n"
    "ds_read_b128 v[16:19], v0 offset:12288\n"
    "v_mul_f32 v164, v221, v164\n"
    "v_mfma_f32_16x16x32_bf16 v[72:75], v[20:23], a[196:199], v[72:75]\n"
    "ds_read_b128 v[20:23], v1 offset:12288\n"
    "v_mul_f32 v165, v221, v165\n"
    "v_mfma_f32_16x16x32_bf16 v[72:75], v[24:27], a[200:203], v[72:75]\n"
    "v_cvt_pk_bf16_f32 v176, v68, v69\n"
    "v_cvt_pk_bf16_f32 v177, v70, v71\n"
    "v_mfma_f32_16x16x32_bf16 v[72:75], v[28:31], a[204:207], v[72:75]\n"
    "ds_read_b128 v[24:27], v0 offset:14336\n"
    "v_mul_f32 v166, v221, v166\n"
    "v_mfma_f32_16x16x32_bf16 v[72:75], v[32:35], a[208:211], v[72:75]\n"
    "ds_read_b128 v[28:31], v1 offset:14336\n"
    "v_mul_f32 v167, v221, v167\n"
    "v_mfma_f32_16x16x32_bf16 v[72:75], v[36:39], a[212:215], v[72:75]\n"
    "ds_read_b128 v[32:35], v0 offset:16384\n"
    "v_cvt_pk_bf16_f32 v108, v160, v161\n"
    "s_waitcnt lgkmcnt(5)\n"
    "v_mfma_f32_16x16x32_bf16 v[64:67], a[72:75], v[40:43], 0\n"
    "ds_read_b128 v[36:39], v1 offset:16384\n"
    "v_cvt_pk_bf16_f32 v109, v162, v163\n"
    "v_mfma_f32_16x16x32_bf16 v[64:67], a[76:79], v[44:47], v[64:67]\n"
    "v_cvt_pk_bf16_f32 v110, v164, v165\n"
    "v_cvt_pk_bf16_f32 v111, v166, v167\n"
    "v_mfma_f32_16x16x32_bf16 v[64:67], a[80:83], v[48:51], v[64:67]\n"
    "v_cvt_pk_bf16_f32 v184, v72, v73\n"
    "v_cvt_pk_bf16_f32 v185, v74, v75\n"
    "v_mfma_f32_16x16x32_bf16 v[64:67], a[84:87], v[52:55], v[64:67]\n"
    "ds_write_b128 v4, v[108:111] offset:6144\n"
    "v_mfma_f32_16x16x32_bf16 v[64:67], a[88:91], v[56:59], v[64:67]\n"
    "v_rcp_f32 v221, v212\n"
    "v_mfma_f32_16x16x32_bf16 v[64:67], a[92:95], v[60:63], v[64:67]\n"
    "v_mul_f32 v192, v221, v192\n"
    "v_mul_f32 v193, v221, v193\n"
    "v_mfma_f32_16x16x32_bf16 v[68:71], a[96:99], v[40:43], 0\n"
    "v_mul_f32 v194, v221, v194\n"
    "v_mul_f32 v195, v221, v195\n"
    "v_mfma_f32_16x16x32_bf16 v[68:71], a[100:103], v[44:47], v[68:71]\n"
    "v_cvt_pk_bf16_f32 v108, v192, v193\n"
    "v_cvt_pk_bf16_f32 v109, v194, v195\n"
    "v_mfma_f32_16x16x32_bf16 v[68:71], a[104:107], v[48:51], v[68:71]\n"
    "v_cvt_pk_bf16_f32 v128, v64, v65\n"
    "v_cvt_pk_bf16_f32 v129, v66, v67\n"
    "v_mfma_f32_16x16x32_bf16 v[68:71], a[108:111], v[52:55], v[68:71]\n"
    "ds_read_b128 v[192:195], v216 offset:0\n"
    "v_mul_f32 v196, v221, v196\n"
    "v_mfma_f32_16x16x32_bf16 v[68:71], a[112:115], v[56:59], v[68:71]\n"
    "v_mul_f32 v197, v221, v197\n"
    "v_mul_f32 v198, v221, v198\n"
    "v_mfma_f32_16x16x32_bf16 v[68:71], a[116:119], v[60:63], v[68:71]\n"
    "v_mul_f32 v199, v221, v199\n"
    "v_cvt_pk_bf16_f32 v110, v196, v197\n"
    "v_mfma_f32_16x16x32_bf16 v[72:75], a[120:123], v[40:43], 0\n"
    "v_cvt_pk_bf16_f32 v111, v198, v199\n"
    "ds_read_b128 v[196:199], v217 offset:0\n"
    "v_mfma_f32_16x16x32_bf16 v[72:75], a[124:127], v[44:47], v[72:75]\n"
    "ds_read_b128 v[200:203], v216 offset:2048\n"
    "v_mul_f32 v208, v221, v208\n"
    "v_mfma_f32_16x16x32_bf16 v[72:75], a[128:131], v[48:51], v[72:75]\n"
    "v_cvt_pk_bf16_f32 v130, v68, v69\n"
    "v_cvt_pk_bf16_f32 v131, v70, v71\n"
    "v_mfma_f32_16x16x32_bf16 v[72:75], a[132:135], v[52:55], v[72:75]\n"
    "ds_write_b64 v5, v[112:113] offset:6144\n"
    "v_mfma_f32_16x16x32_bf16 v[72:75], a[136:139], v[56:59], v[72:75]\n"
    "ds_read_b128 v[204:207], v217 offset:2048\n"
    "v_mul_f32 v209, v221, v209\n"
    "v_mfma_f32_16x16x32_bf16 v[72:75], a[140:143], v[60:63], v[72:75]\n"
    "v_mul_f32 v210, v221, v210\n"
    "v_mul_f32 v211, v221, v211\n"
    "v_mfma_f32_16x16x32_bf16 v[64:67], v[40:43], a[144:147], 0\n"
    "v_cvt_pk_bf16_f32 v112, v208, v209\n"
    "v_cvt_pk_bf16_f32 v113, v210, v211\n"
    "v_mfma_f32_16x16x32_bf16 v[64:67], v[44:47], a[148:151], v[64:67]\n"
    "ds_read_b128 v[208:211], v216 offset:4096\n"
    "s_lshl_b32 s90, s15, 11\n"
    "v_mfma_f32_16x16x32_bf16 v[64:67], v[48:51], a[152:155], v[64:67]\n"
    "v_cvt_pk_bf16_f32 v144, v72, v73\n"
    "v_cvt_pk_bf16_f32 v145, v74, v75\n"
    "v_mfma_f32_16x16x32_bf16 v[64:67], v[52:55], a[156:159], v[64:67]\n"
    "ds_read_b128 v[212:215], v217 offset:4096\n"
    "v_add_u32 v218, s93, v3\n"
    "v_mfma_f32_16x16x32_bf16 v[64:67], v[56:59], a[160:163], v[64:67]\n"
    "ds_write_b128 v4, v[116:119] offset:0\n"
    "v_mfma_f32_16x16x32_bf16 v[64:67], v[60:63], a[164:167], v[64:67]\n"
    "v_add_u32 v216, s90, v0\n"
    "ds_read_b128 v[94:97], v216 offset:24576\n"
    "v_mfma_f32_16x16x32_bf16 v[68:71], v[40:43], a[168:171], 0\n"
    "ds_read_b128 v[152:155], v218 offset:0\n"
    "v_mul_f32 v84, v220, v84\n"
    "v_mfma_f32_16x16x32_bf16 v[68:71], v[44:47], a[172:175], v[68:71]\n"
    "v_mul_f32 v85, v220, v85\n"
    "v_mul_f32 v86, v220, v86\n"
    "v_mfma_f32_16x16x32_bf16 v[68:71], v[48:51], a[176:179], v[68:71]\n"
    "v_cvt_pk_bf16_f32 v170, v64, v65\n"
    "v_cvt_pk_bf16_f32 v171, v66, v67\n"
    "v_mfma_f32_16x16x32_bf16 v[68:71], v[52:55], a[180:183], v[68:71]\n"
    "v_mul_f32 v87, v220, v87\n"
    "v_mul_f32 v88, v220, v88\n"
    "v_mfma_f32_16x16x32_bf16 v[68:71], v[56:59], a[184:187], v[68:71]\n"
    "v_mul_f32 v89, v220, v89\n"
    "v_mul_f32 v90, v220, v90\n"
    "v_mfma_f32_16x16x32_bf16 v[68:71], v[60:63], a[188:191], v[68:71]\n"
    "v_mul_f32 v91, v220, v91\n"
    "v_cvt_pk_bf16_f32 v116, v84, v85\n"
    "v_mfma_f32_16x16x32_bf16 v[72:75], v[40:43], a[192:195], 0\n"
    "ds_read_b128 v[40:43], v0 offset:18432\n"
    "v_cvt_pk_bf16_f32 v117, v86, v87\n"
    "v_mfma_f32_16x16x32_bf16 v[72:75], v[44:47], a[196:199], v[72:75]\n"
    "ds_read_b128 v[44:47], v1 offset:18432\n"
    "v_cvt_pk_bf16_f32 v118, v88, v89\n"
    "v_mfma_f32_16x16x32_bf16 v[72:75], v[48:51], a[200:203], v[72:75]\n"
    "v_cvt_pk_bf16_f32 v178, v68, v69\n"
    "v_cvt_pk_bf16_f32 v179, v70, v71\n"
    "v_mfma_f32_16x16x32_bf16 v[72:75], v[52:55], a[204:207], v[72:75]\n"
    "ds_read_b128 v[48:51], v0 offset:20480\n"
    "v_cvt_pk_bf16_f32 v119, v90, v91\n"
    "v_mfma_f32_16x16x32_bf16 v[72:75], v[56:59], a[208:211], v[72:75]\n"
    "ds_read_b128 v[52:55], v1 offset:20480\n"
    "v_add_u32 v217, s90, v1\n"
    "v_mfma_f32_16x16x32_bf16 v[72:75], v[60:63], a[212:215], v[72:75]\n"
    "ds_read_b128 v[56:59], v0 offset:22528\n"
    "s_and_b32 s91, s78, 32768\n"
    "s_waitcnt lgkmcnt(5)\n"
    "v_mfma_f32_16x16x32_bf16 v[64:67], a[72:75], v[16:19], 0\n"
    "ds_read_b128 v[60:63], v1 offset:22528\n"
    "s_mov_b32 s18, s22\n"
    "v_mfma_f32_16x16x32_bf16 v[64:67], a[76:79], v[20:23], v[64:67]\n"
    "ds_write_b128 v4, v[116:119] offset:12288\n"
    "v_mfma_f32_16x16x32_bf16 v[64:67], a[80:83], v[24:27], v[64:67]\n"
    "v_cvt_pk_bf16_f32 v186, v72, v73\n"
    "v_cvt_pk_bf16_f32 v187, v74, v75\n"
    "v_mfma_f32_16x16x32_bf16 v[64:67], a[84:87], v[28:31], v[64:67]\n"
    "ds_write_b64 v5, v[120:121] offset:12288\n"
    "v_mfma_f32_16x16x32_bf16 v[64:67], a[88:91], v[32:35], v[64:67]\n"
    "ds_write_b128 v4, v[108:111] offset:18432\n"
    "v_mfma_f32_16x16x32_bf16 v[64:67], a[92:95], v[36:39], v[64:67]\n"
    "ds_write_b64 v5, v[112:113] offset:18432\n"
    "v_mfma_f32_16x16x32_bf16 v[68:71], a[96:99], v[16:19], 0\n"
    "ds_read_b128 v[156:159], v218 offset:1024\n"
    "s_mov_b32 s16, s20\n"
    "v_mfma_f32_16x16x32_bf16 v[68:71], a[100:103], v[20:23], v[68:71]\n"
    "ds_read_b128 v[102:105], v217 offset:24576\n"
    "s_mov_b32 s17, s21\n"
    "v_mfma_f32_16x16x32_bf16 v[68:71], a[104:107], v[24:27], v[68:71]\n"
    "v_cvt_pk_bf16_f32 v132, v64, v65\n"
    "v_cvt_pk_bf16_f32 v133, v66, v67\n"
    "v_mfma_f32_16x16x32_bf16 v[68:71], a[108:111], v[28:31], v[68:71]\n"
    "ds_read_b128 v[160:163], v218 offset:2048\n"
    "s_mov_b32 s19, s23\n"
    "v_mfma_f32_16x16x32_bf16 v[68:71], a[112:115], v[32:35], v[68:71]\n"
    "ds_read_b128 v[164:167], v218 offset:3072\n"
    "v_mbcnt_lo_u32_b32 v82, -1, 0\n"
    "v_mfma_f32_16x16x32_bf16 v[68:71], a[116:119], v[36:39], v[68:71]\n"
    "s_cmp_ge_u32 s15, 2\n"
    "s_cselect_b32 s48, s50, s48\n"
    "s_cselect_b32 s49, s51, s49\n"
    "v_mfma_f32_16x16x32_bf16 v[72:75], a[120:123], v[16:19], 0\n"
    "s_waitcnt lgkmcnt(4)\n"
    "s_barrier\n"
    "v_mfma_f32_16x16x32_bf16 v[72:75], a[124:127], v[20:23], v[72:75]\n"
    "s_cmp_eq_u32 s77, 3\n"
    "s_cselect_b32 s18, 0, s18\n"
    "v_mfma_f32_16x16x32_bf16 v[72:75], a[128:131], v[24:27], v[72:75]\n"
    "s_bitcmp1_b32 s15, 0\n"
    "s_cselect_b32 s48, s49, s48\n"
    "v_mfma_f32_16x16x32_bf16 v[72:75], a[132:135], v[28:31], v[72:75]\n"
    "v_cvt_pk_bf16_f32 v134, v68, v69\n"
    "v_cvt_pk_bf16_f32 v135, v70, v71\n"
    "v_mfma_f32_16x16x32_bf16 v[72:75], a[136:139], v[32:35], v[72:75]\n"
    "v_mbcnt_hi_u32_b32 v82, -1, v82\n"
    "v_and_b32 v83, 15, v82\n"
    "v_mfma_f32_16x16x32_bf16 v[72:75], a[140:143], v[36:39], v[72:75]\n"
    "v_lshrrev_b32 v219, 4, v82\n"
    "v_and_b32 v223, 7, v82\n"
    "v_mfma_f32_16x16x32_bf16 v[64:67], v[16:19], a[144:147], 0\n"
    "v_lshrrev_b32 v81, 3, v82\n"
    "s_mul_i32 s88, s77, 6\n"
    "v_mfma_f32_16x16x32_bf16 v[64:67], v[20:23], a[148:151], v[64:67]\n"
    "v_add_u32 v80, s88, v219\n"
    "v_lshrrev_b32 v8, 1, v219\n"
    "v_mfma_f32_16x16x32_bf16 v[64:67], v[24:27], a[152:155], v[64:67]\n"
    "v_cvt_pk_bf16_f32 v146, v72, v73\n"
    "v_cvt_pk_bf16_f32 v147, v74, v75\n"
    "v_mfma_f32_16x16x32_bf16 v[64:67], v[28:31], a[156:159], v[64:67]\n"
    "s_add_u32 s88, s88, 4\n"
    "v_add_u32 v8, s88, v8\n"
    "v_mfma_f32_16x16x32_bf16 v[64:67], v[32:35], a[160:163], v[64:67]\n"
    "v_xor_b32 v80, v80, v223\n"
    "v_mul_u32_u24 v83, 384, v83\n"
    "v_mfma_f32_16x16x32_bf16 v[64:67], v[36:39], a[164:167], v[64:67]\n"
    "v_xor_b32 v8, v8, v223\n"
    "v_lshl_add_u32 v80, v80, 4, v83\n"
    "v_mfma_f32_16x16x32_bf16 v[68:71], v[16:19], a[168:171], 0\n"
    "v_lshl_add_u32 v8, v8, 4, v83\n"
    "v_add_u32 v80, s91, v80\n"
    "v_mfma_f32_16x16x32_bf16 v[68:71], v[20:23], a[172:175], v[68:71]\n"
    "v_and_b32 v9, 1, v219\n"
    "v_lshl_add_u32 v8, v9, 3, v8\n"
    "v_mfma_f32_16x16x32_bf16 v[68:71], v[24:27], a[176:179], v[68:71]\n"
    "v_cvt_pk_bf16_f32 v172, v64, v65\n"
    "v_cvt_pk_bf16_f32 v173, v66, v67\n"
    "v_mfma_f32_16x16x32_bf16 v[68:71], v[28:31], a[180:183], v[68:71]\n"
    "v_add_u32 v8, s91, v8\n"
    "v_xor_b32 v9, v223, v219\n"
    "v_mfma_f32_16x16x32_bf16 v[68:71], v[32:35], a[184:187], v[68:71]\n"
    "v_xor_b32 v9, v9, v81\n"
    "v_mul_u32_u24 v81, 384, v81\n"
    "v_mfma_f32_16x16x32_bf16 v[68:71], v[36:39], a[188:191], v[68:71]\n"
    "s_mul_i32 s89, s77, 6144\n"
    "v_lshl_add_u32 v9, v9, 4, v81\n"
    "v_mfma_f32_16x16x32_bf16 v[72:75], v[16:19], a[192:195], 0\n"
    "ds_read_b128 v[16:19], v2 offset:0\n"
    "v_add_u32 v9, s89, v9\n"
    "v_mfma_f32_16x16x32_bf16 v[72:75], v[20:23], a[196:199], v[72:75]\n"
    "ds_read_b128 v[20:23], v2 offset:1024\n"
    "v_xor_b32 v81, 64, v9\n"
    "v_mfma_f32_16x16x32_bf16 v[72:75], v[24:27], a[200:203], v[72:75]\n"
    "v_cvt_pk_bf16_f32 v180, v68, v69\n"
    "v_cvt_pk_bf16_f32 v181, v70, v71\n"
    "v_mfma_f32_16x16x32_bf16 v[72:75], v[28:31], a[204:207], v[72:75]\n"
    "ds_read_b128 v[24:27], v2 offset:2048\n"
    "v_add_u32 v9, s91, v9\n"
    "v_mfma_f32_16x16x32_bf16 v[72:75], v[32:35], a[208:211], v[72:75]\n"
    "ds_read_b128 v[28:31], v2 offset:3072\n"
    "v_add_u32 v81, s91, v81\n"
    "v_mfma_f32_16x16x32_bf16 v[72:75], v[36:39], a[212:215], v[72:75]\n"
    "ds_read_b128 v[32:35], v2 offset:4096\n"
    "v_mfma_f32_16x16x32_bf16 v[64:67], a[72:75], v[40:43], 0\n"
    "ds_read_b128 v[36:39], v2 offset:5120\n"
    "v_mfma_f32_16x16x32_bf16 v[64:67], a[76:79], v[44:47], v[64:67]\n"
    "s_cmp_ge_u32 s77, 2\n"
    "s_cselect_b32 s44, s46, s44\n"
    "s_cselect_b32 s45, s47, s45\n"
    "v_mfma_f32_16x16x32_bf16 v[64:67], a[80:83], v[48:51], v[64:67]\n"
    "s_bitcmp1_b32 s77, 0\n"
    "s_cselect_b32 s44, s45, s44\n"
    "v_mfma_f32_16x16x32_bf16 v[64:67], a[84:87], v[52:55], v[64:67]\n"
    "v_cvt_pk_bf16_f32 v188, v72, v73\n"
    "v_cvt_pk_bf16_f32 v189, v74, v75\n"
    "v_mfma_f32_16x16x32_bf16 v[64:67], a[88:91], v[56:59], v[64:67]\n"
    "v_mfma_f32_16x16x32_bf16 v[64:67], a[92:95], v[60:63], v[64:67]\n"
    "v_mfma_f32_16x16x32_bf16 v[68:71], a[96:99], v[40:43], 0\n"
    "v_mfma_f32_16x16x32_bf16 v[68:71], a[100:103], v[44:47], v[68:71]\n"
    "v_mfma_f32_16x16x32_bf16 v[68:71], a[104:107], v[48:51], v[68:71]\n"
    "v_mfma_f32_16x16x32_bf16 v[68:71], a[108:111], v[52:55], v[68:71]\n"
    "v_mfma_f32_16x16x32_bf16 v[68:71], a[112:115], v[56:59], v[68:71]\n"
    "v_mfma_f32_16x16x32_bf16 v[68:71], a[116:119], v[60:63], v[68:71]\n"
    "v_mfma_f32_16x16x32_bf16 v[72:75], a[120:123], v[40:43], 0\n"
    "s_nop 0\n"
    "v_cvt_pk_bf16_f32 v136, v64, v65\n"
    "v_mfma_f32_16x16x32_bf16 v[72:75], a[124:127], v[44:47], v[72:75]\n"
    "v_cvt_pk_bf16_f32 v137, v66, v67\n"
    "v_mfma_f32_16x16x32_bf16 v[72:75], a[128:131], v[48:51], v[72:75]\n"
    "v_mfma_f32_16x16x32_bf16 v[72:75], a[132:135], v[52:55], v[72:75]\n"
    "s_nop 0\n"
    "v_cvt_pk_bf16_f32 v138, v68, v69\n"
    "v_mfma_f32_16x16x32_bf16 v[72:75], a[136:139], v[56:59], v[72:75]\n"
    "v_cvt_pk_bf16_f32 v139, v70, v71\n"
    "v_mfma_f32_16x16x32_bf16 v[72:75], a[140:143], v[60:63], v[72:75]\n"
    "v_mfma_f32_16x16x32_bf16 v[64:67], v[40:43], a[144:147], 0\n"
    "v_mfma_f32_16x16x32_bf16 v[64:67], v[44:47], a[148:151], v[64:67]\n"
    "v_mfma_f32_16x16x32_bf16 v[64:67], v[48:51], a[152:155], v[64:67]\n"
    "v_mfma_f32_16x16x32_bf16 v[64:67], v[52:55], a[156:159], v[64:67]\n"
    "v_mfma_f32_16x16x32_bf16 v[64:67], v[56:59], a[160:163], v[64:67]\n"
    "v_mfma_f32_16x16x32_bf16 v[64:67], v[60:63], a[164:167], v[64:67]\n"
    "v_mfma_f32_16x16x32_bf16 v[68:71], v[40:43], a[168:171], 0\n"
    "s_nop 0\n"
    "v_cvt_pk_bf16_f32 v150, v72, v73\n"
    "v_mfma_f32_16x16x32_bf16 v[68:71], v[44:47], a[172:175], v[68:71]\n"
    "v_cvt_pk_bf16_f32 v151, v74, v75\n"
    "v_mfma_f32_16x16x32_bf16 v[68:71], v[48:51], a[176:179], v[68:71]\n"
    "v_mfma_f32_16x16x32_bf16 v[68:71], v[52:55], a[180:183], v[68:71]\n"
    "s_nop 0\n"
    "v_cvt_pk_bf16_f32 v174, v64, v65\n"
    "v_mfma_f32_16x16x32_bf16 v[68:71], v[56:59], a[184:187], v[68:71]\n"
    "v_cvt_pk_bf16_f32 v175, v66, v67\n"
    "v_mfma_f32_16x16x32_bf16 v[68:71], v[60:63], a[188:191], v[68:71]\n"
    "v_mfma_f32_16x16x32_bf16 v[72:75], v[40:43], a[192:195], 0\n"
    "ds_read_b128 v[40:43], v2 offset:6144\n"
    "v_mfma_f32_16x16x32_bf16 v[72:75], v[44:47], a[196:199], v[72:75]\n"
    "ds_read_b128 v[44:47], v2 offset:7168\n"
    "v_mfma_f32_16x16x32_bf16 v[72:75], v[48:51], a[200:203], v[72:75]\n"
    "ds_read_b128 v[48:51], v2 offset:8192\n"
    "v_mfma_f32_16x16x32_bf16 v[72:75], v[52:55], a[204:207], v[72:75]\n"
    "ds_read_b128 v[52:55], v2 offset:9216\n"
    "v_cvt_pk_bf16_f32 v182, v68, v69\n"
    "v_mfma_f32_16x16x32_bf16 v[72:75], v[56:59], a[208:211], v[72:75]\n"
    "v_cvt_pk_bf16_f32 v183, v70, v71\n"
    "ds_read_b128 v[56:59], v2 offset:10240\n"
    "v_mfma_f32_16x16x32_bf16 v[72:75], v[60:63], a[212:215], v[72:75]\n"
    "ds_read_b128 v[60:63], v2 offset:11264\n"
    "v_mfma_f32_16x16x32_bf16 v[64:67], a[0:3], v[192:195], 0\n"
    "v_mfma_f32_16x16x32_bf16 v[64:67], a[4:7], v[196:199], v[64:67]\n"
    "v_mfma_f32_16x16x32_bf16 v[64:67], a[8:11], v[200:203], v[64:67]\n"
    "v_mfma_f32_16x16x32_bf16 v[64:67], a[12:15], v[204:207], v[64:67]\n"
    "v_mfma_f32_16x16x32_bf16 v[64:67], a[16:19], v[208:211], v[64:67]\n"
    "v_mfma_f32_16x16x32_bf16 v[64:67], a[20:23], v[212:215], v[64:67]\n"
    "s_nop 0\n"
    "v_cvt_pk_bf16_f32 v190, v72, v73\n"
    "v_mfma_f32_16x16x32_bf16 v[68:71], a[24:27], v[192:195], 0\n"
    "v_cvt_pk_bf16_f32 v191, v74, v75\n"
    "v_mfma_f32_16x16x32_bf16 v[68:71], a[28:31], v[196:199], v[68:71]\n"
    "v_mfma_f32_16x16x32_bf16 v[68:71], a[32:35], v[200:203], v[68:71]\n"
    "v_mfma_f32_16x16x32_bf16 v[68:71], a[36:39], v[204:207], v[68:71]\n"
    "s_nop 0\n"
    "v_mul_f32 v64, s75, v64\n"
    "v_mfma_f32_16x16x32_bf16 v[68:71], a[40:43], v[208:211], v[68:71]\n"
    "v_mul_f32 v65, s75, v65\n"
    "v_mul_f32 v66, s75, v66\n"
    "v_mfma_f32_16x16x32_bf16 v[68:71], a[44:47], v[212:215], v[68:71]\n"
    "v_mul_f32 v67, s75, v67\n"
    "v_cvt_pk_bf16_f32 v76, v64, v65\n"
    "v_mfma_f32_16x16x32_bf16 v[72:75], a[48:51], v[192:195], 0\n"
    "v_cvt_pk_bf16_f32 v77, v66, v67\n"
    "v_mfma_f32_16x16x32_bf16 v[72:75], a[52:55], v[196:199], v[72:75]\n"
    "v_mfma_f32_16x16x32_bf16 v[72:75], a[56:59], v[200:203], v[72:75]\n"
    "v_mfma_f32_16x16x32_bf16 v[72:75], a[60:63], v[204:207], v[72:75]\n"
    "s_nop 0\n"
    "v_mul_f32 v68, s75, v68\n"
    "v_mfma_f32_16x16x32_bf16 v[72:75], a[64:67], v[208:211], v[72:75]\n"
    "v_mul_f32 v69, s75, v69\n"
    "v_mul_f32 v70, s75, v70\n"
    "v_mfma_f32_16x16x32_bf16 v[72:75], a[68:71], v[212:215], v[72:75]\n"
    "v_mul_f32 v71, s75, v71\n"
    "v_cvt_pk_bf16_f32 v78, v68, v69\n"
    "v_cvt_pk_bf16_f32 v79, v70, v71\n"
    "s_nop 0\n"
    "s_nop 0\n"
    "s_nop 0\n"
    "s_nop 0\n"
    "s_nop 0\n"
    "v_mul_f32 v72, s75, v72\n"
    "v_mul_f32 v73, s75, v73\n"
    "v_mul_f32 v74, s75, v74\n"
    "v_mul_f32 v75, s75, v75\n"
    "v_cvt_pk_bf16_f32 v92, v72, v73\n"
    "v_cvt_pk_bf16_f32 v93, v74, v75\n"
    "v_cvt_pk_bf16_f32 v98, v72, v73\n"
    "v_cvt_pk_bf16_f32 v99, v74, v75\n"
    "v_mfma_f32_16x16x32_bf16 v[152:155], v[140:143], v[92:95], v[152:155]\n"
    "v_cvt_pk_bf16_f32 v100, v72, v73\n"
    "v_cvt_pk_bf16_f32 v101, v74, v75\n"
    "v_mfma_f32_16x16x32_bf16 v[152:155], v[124:127], v[76:79], v[152:155]\n"
    "v_cvt_pk_bf16_f32 v106, v72, v73\n"
    "v_cvt_pk_bf16_f32 v107, v74, v75\n"
    "s_waitcnt lgkmcnt(0)\n"
    "v_mfma_f32_16x16x32_bf16 v[156:159], v[142:145], v[96:99], v[156:159]\n"
    "v_mfma_f32_16x16x32_bf16 v[156:159], v[128:131], v[76:79], v[156:159]\n"
    "v_mfma_f32_16x16x32_bf16 v[160:163], v[146:149], v[100:103], v[160:163]\n"
    "v_mfma_f32_16x16x32_bf16 v[160:163], v[132:135], v[76:79], v[160:163]\n"
    "v_mfma_f32_16x16x32_bf16 v[164:167], v[148:151], v[104:107], v[164:167]\n"
    "s_nop 0\n"
    "v_max3_f32 v82, v152, v153, v154\n"
    "v_mfma_f32_16x16x32_bf16 v[164:167], v[136:139], v[76:79], v[164:167]\n"
    "v_mfma_f32_16x16x32_bf16 v[64:67], a[216:219], v[16:19], 0\n"
    "s_nop 0\n"
    "v_max3_f32 v82, v82, v155, v156\n"
    "v_mfma_f32_16x16x32_bf16 v[64:67], a[220:223], v[20:23], v[64:67]\n"
    "v_max3_f32 v82, v82, v157, v158\n"
    "v_max3_f32 v82, v82, v159, v160\n"
    "v_mfma_f32_16x16x32_bf16 v[64:67], a[224:227], v[24:27], v[64:67]\n"
    "v_max3_f32 v82, v82, v161, v162\n"
    "v_max3_f32 v82, v82, v163, v164\n"
    "v_mfma_f32_16x16x32_bf16 v[64:67], a[228:231], v[28:31], v[64:67]\n"
    "v_max3_f32 v82, v82, v165, v166\n"
    "v_max_f32 v82, v82, v167\n"
    "v_mfma_f32_16x16x32_bf16 v[64:67], a[232:235], v[32:35], v[64:67]\n"
    "v_mov_b32 v83, v82\n"
    "v_mfma_f32_16x16x32_bf16 v[64:67], a[236:239], v[36:39], v[64:67]\n"
    "s_nop 0\n"
    "v_permlane32_swap_b32 v82, v83\n"
    "v_mfma_f32_16x16x32_bf16 v[68:71], a[240:243], v[16:19], 0\n"
    "v_max_f32 v82, v82, v83\n"
    "v_mov_b32 v83, v82\n"
    "v_mfma_f32_16x16x32_bf16 v[68:71], a[244:247], v[20:23], v[68:71]\n"
    "s_nop 0\n"
    "v_permlane16_swap_b32 v82, v83\n"
    "v_mfma_f32_16x16x32_bf16 v[68:71], a[248:251], v[24:27], v[68:71]\n"
    "v_cvt_pk_bf16_f32 v108, v64, v65\n"
    "v_cvt_pk_bf16_f32 v109, v66, v67\n"
    "v_mfma_f32_16x16x32_bf16 v[68:71], a[252:255], v[28:31], v[68:71]\n"
    "v_max_f32 v82, v82, v83\n"
    "v_mul_f32 v82, s74, v82\n"
    "v_mfma_f32_16x16x32_bf16 v[68:71], v[224:227], v[32:35], v[68:71]\n"
    "v_fma_f32 v152, v152, s73, v82\n"
    "v_fma_f32 v153, v153, s73, v82\n"
    "v_mfma_f32_16x16x32_bf16 v[68:71], v[228:231], v[36:39], v[68:71]\n"
    "v_exp_f32 v152, v152\n"
    "v_mfma_f32_16x16x32_bf16 v[72:75], v[232:235], v[16:19], 0\n"
    "v_exp_f32 v153, v153\n"
    "v_mfma_f32_16x16x32_bf16 v[72:75], v[236:239], v[20:23], v[72:75]\n"
    "v_fma_f32 v154, v154, s73, v82\n"
    "v_fma_f32 v155, v155, s73, v82\n"
    "v_mfma_f32_16x16x32_bf16 v[72:75], v[240:243], v[24:27], v[72:75]\n"
    "v_exp_f32 v154, v154\n"
    "v_mfma_f32_16x16x32_bf16 v[72:75], v[244:247], v[28:31], v[72:75]\n"
    "v_cvt_pk_bf16_f32 v110, v68, v69\n"
    "v_cvt_pk_bf16_f32 v111, v70, v71\n"
    "v_mfma_f32_16x16x32_bf16 v[72:75], v[248:251], v[32:35], v[72:75]\n"
    "ds_write_b128 v80, v[108:111] offset:0\n"
    "v_mfma_f32_16x16x32_bf16 v[72:75], v[252:255], v[36:39], v[72:75]\n"
    "v_exp_f32 v155, v155\n"
    "v_mfma_f32_16x16x32_bf16 v[64:67], a[216:219], v[40:43], 0\n"
    "v_fma_f32 v156, v156, s73, v82\n"
    "v_fma_f32 v157, v157, s73, v82\n"
    "v_mfma_f32_16x16x32_bf16 v[64:67], a[220:223], v[44:47], v[64:67]\n"
    "v_fma_f32 v158, v158, s73, v82\n"
    "v_fma_f32 v159, v159, s73, v82\n"
    "v_mfma_f32_16x16x32_bf16 v[64:67], a[224:227], v[48:51], v[64:67]\n"
    "v_cvt_pk_bf16_f32 v112, v72, v73\n"
    "v_cvt_pk_bf16_f32 v113, v74, v75\n"
    "v_mfma_f32_16x16x32_bf16 v[64:67], a[228:231], v[52:55], v[64:67]\n"
    "ds_write_b64 v8, v[112:113] offset:0\n"
    "v_mfma_f32_16x16x32_bf16 v[64:67], a[232:235], v[56:59], v[64:67]\n"
    "v_cvt_pk_bf16_f32 v152, v152, v153\n"
    "v_cvt_pk_bf16_f32 v153, v154, v155\n"
    "v_mfma_f32_16x16x32_bf16 v[64:67], a[236:239], v[60:63], v[64:67]\n"
    "v_exp_f32 v156, v156\n"
    "v_mfma_f32_16x16x32_bf16 v[68:71], a[240:243], v[40:43], 0\n"
    "v_exp_f32 v157, v157\n"
    "v_mfma_f32_16x16x32_bf16 v[68:71], a[244:247], v[44:47], v[68:71]\n"
    "v_exp_f32 v158, v158\n"
    "v_mfma_f32_16x16x32_bf16 v[68:71], a[248:251], v[48:51], v[68:71]\n"
    "v_exp_f32 v159, v159\n"
    "v_mfma_f32_16x16x32_bf16 v[68:71], a[252:255], v[52:55], v[68:71]\n"
    "v_cvt_pk_bf16_f32 v154, v156, v157\n"
    "v_cvt_pk_bf16_f32 v155, v158, v159\n"
    "v_mfma_f32_16x16x32_bf16 v[68:71], v[224:227], v[56:59], v[68:71]\n"
    "v_fma_f32 v160, v160, s73, v82\n"
    "v_fma_f32 v161, v161, s73, v82\n"
    "v_mfma_f32_16x16x32_bf16 v[68:71], v[228:231], v[60:63], v[68:71]\n"
    "v_fma_f32 v162, v162, s73, v82\n"
    "v_fma_f32 v163, v163, s73, v82\n"
    "v_mfma_f32_16x16x32_bf16 v[72:75], v[232:235], v[40:43], 0\n"
    "v_fma_f32 v164, v164, s73, v82\n"
    "v_fma_f32 v165, v165, s73, v82\n"
    "v_mfma_f32_16x16x32_bf16 v[72:75], v[236:239], v[44:47], v[72:75]\n"
    "v_fma_f32 v166, v166, s73, v82\n"
    "v_fma_f32 v167, v167, s73, v82\n"
    "v_mfma_f32_16x16x32_bf16 v[72:75], v[240:243], v[48:51], v[72:75]\n"
    "v_exp_f32 v160, v160\n"
    "v_mfma_f32_16x16x32_bf16 v[72:75], v[244:247], v[52:55], v[72:75]\n"
    "v_exp_f32 v161, v161\n"
    "v_mfma_f32_16x16x32_bf16 v[72:75], v[248:251], v[56:59], v[72:75]\n"
    "v_exp_f32 v162, v162\n"
    "v_mfma_f32_16x16x32_bf16 v[72:75], v[252:255], v[60:63], v[72:75]\n"
    "v_exp_f32 v163, v163\n"
    "v_mfma_f32_16x16x32_bf16 v[136:139], v[12:15], v[152:155], 0\n"
    "v_exp_f32 v164, v164\n"
    "v_exp_f32 v165, v165\n"
    "v_exp_f32 v166, v166\n"
    "v_exp_f32 v167, v167\n"
    "v_cvt_pk_bf16_f32 v156, v160, v161\n"
    "v_cvt_pk_bf16_f32 v157, v162, v163\n"
    "v_cvt_pk_bf16_f32 v158, v164, v165\n"
    "v_cvt_pk_bf16_f32 v159, v166, v167\n"
    "ds_read_b128 v[16:19], v2 offset:12288\n"
    "v_cvt_pk_bf16_f32 v116, v64, v65\n"
    "v_mfma_f32_16x16x32_bf16 v[136:139], v[12:15], v[156:159], v[136:139]\n"
    "ds_read_b128 v[20:23], v2 offset:13312\n"
    "v_cvt_pk_bf16_f32 v117, v66, v67\n"
    "v_mfma_f32_16x16x32_bf16 v[124:127], v[168:171], v[152:155], 0\n"
    "ds_read_b128 v[24:27], v2 offset:14336\n"
    "v_cvt_pk_bf16_f32 v118, v68, v69\n"
    "v_mfma_f32_16x16x32_bf16 v[124:127], v[172:175], v[156:159], v[124:127]\n"
    "ds_read_b128 v[28:31], v2 offset:15360\n"
    "v_cvt_pk_bf16_f32 v119, v70, v71\n"
    "v_mfma_f32_16x16x32_bf16 v[128:131], v[176:179], v[152:155], 0\n"
    "ds_write_b128 v80, v[116:119] offset:6144\n"
    "v_mfma_f32_16x16x32_bf16 v[128:131], v[180:183], v[156:159], v[128:131]\n"
    "ds_read_b128 v[32:35], v2 offset:16384\n"
    "v_cvt_pk_bf16_f32 v120, v72, v73\n"
    "v_mfma_f32_16x16x32_bf16 v[132:135], v[184:187], v[152:155], 0\n"
    "ds_read_b128 v[36:39], v2 offset:17408\n"
    "v_cvt_pk_bf16_f32 v121, v74, v75\n"
    "v_mfma_f32_16x16x32_bf16 v[132:135], v[188:191], v[156:159], v[132:135]\n"
    "ds_write_b64 v8, v[120:121] offset:6144\n"
    "s_waitcnt lgkmcnt(6)\n"
    "v_mfma_f32_16x16x32_bf16 v[64:67], a[216:219], v[16:19], 0\n"
    "ds_read_b128 v[40:43], v2 offset:18432\n"
    "v_mfma_f32_16x16x32_bf16 v[64:67], a[220:223], v[20:23], v[64:67]\n"
    "ds_read_b128 v[44:47], v2 offset:19456\n"
    "s_waitcnt lgkmcnt(6)\n"
    "v_mfma_f32_16x16x32_bf16 v[64:67], a[224:227], v[24:27], v[64:67]\n"
    "ds_read_b128 v[48:51], v2 offset:20480\n"
    "v_mfma_f32_16x16x32_bf16 v[64:67], a[228:231], v[28:31], v[64:67]\n"
    "ds_read_b128 v[52:55], v2 offset:21504\n"
    "v_rcp_f32 v220, v136\n"
    "ds_read_b128 v[56:59], v2 offset:22528\n"
    "s_waitcnt lgkmcnt(7)\n"
    "v_mfma_f32_16x16x32_bf16 v[64:67], a[232:235], v[32:35], v[64:67]\n"
    "ds_read_b128 v[60:63], v2 offset:23552\n"
    "v_mul_f32 v124, v220, v124\n"
    "s_waitcnt lgkmcnt(7)\n"
    "v_mfma_f32_16x16x32_bf16 v[64:67], a[236:239], v[36:39], v[64:67]\n"
    "v_mul_f32 v125, v220, v125\n"
    "v_mul_f32 v126, v220, v126\n"
    "v_mfma_f32_16x16x32_bf16 v[68:71], a[240:243], v[16:19], 0\n"
    "v_mul_f32 v127, v220, v127\n"
    "v_mul_f32 v128, v220, v128\n"
    "v_mfma_f32_16x16x32_bf16 v[68:71], a[244:247], v[20:23], v[68:71]\n"
    "v_mul_f32 v129, v220, v129\n"
    "v_mul_f32 v130, v220, v130\n"
    "v_mfma_f32_16x16x32_bf16 v[68:71], a[248:251], v[24:27], v[68:71]\n"
    "v_cvt_pk_bf16_f32 v108, v64, v65\n"
    "v_cvt_pk_bf16_f32 v109, v66, v67\n"
    "v_mfma_f32_16x16x32_bf16 v[68:71], a[252:255], v[28:31], v[68:71]\n"
    "v_mul_f32 v131, v220, v131\n"
    "v_mul_f32 v132, v220, v132\n"
    "v_mfma_f32_16x16x32_bf16 v[68:71], v[224:227], v[32:35], v[68:71]\n"
    "v_mul_f32 v133, v220, v133\n"
    "v_mul_f32 v134, v220, v134\n"
    "v_mfma_f32_16x16x32_bf16 v[68:71], v[228:231], v[36:39], v[68:71]\n"
    "v_mul_f32 v135, v220, v135\n"
    "v_cvt_pk_bf16_f32 v92, v124, v125\n"
    "v_mfma_f32_16x16x32_bf16 v[72:75], v[232:235], v[16:19], 0\n"
    "v_cvt_pk_bf16_f32 v93, v126, v127\n"
    "v_cvt_pk_bf16_f32 v94, v128, v129\n"
    "v_mfma_f32_16x16x32_bf16 v[72:75], v[236:239], v[20:23], v[72:75]\n"
    "v_cvt_pk_bf16_f32 v95, v130, v131\n"
    "v_cvt_pk_bf16_f32 v96, v132, v133\n"
    "v_mfma_f32_16x16x32_bf16 v[72:75], v[240:243], v[24:27], v[72:75]\n"
    "ds_write_b128 v4, v[92:95] offset:24576\n"
    "v_mfma_f32_16x16x32_bf16 v[72:75], v[244:247], v[28:31], v[72:75]\n"
    "v_cvt_pk_bf16_f32 v97, v134, v135\n"
    "v_cvt_pk_bf16_f32 v110, v68, v69\n"
    "v_mfma_f32_16x16x32_bf16 v[72:75], v[248:251], v[32:35], v[72:75]\n"
    "ds_write_b64 v5, v[96:97] offset:24576\n"
    "v_mfma_f32_16x16x32_bf16 v[72:75], v[252:255], v[36:39], v[72:75]\n"
    "v_cvt_pk_bf16_f32 v111, v70, v71\n"
    "s_waitcnt lgkmcnt(2)\n"
    "v_mfma_f32_16x16x32_bf16 v[64:67], a[216:219], v[40:43], 0\n"
    "ds_write_b128 v80, v[108:111] offset:12288\n"
    "v_mfma_f32_16x16x32_bf16 v[64:67], a[220:223], v[44:47], v[64:67]\n"
    "v_mfma_f32_16x16x32_bf16 v[64:67], a[224:227], v[48:51], v[64:67]\n"
    "v_mfma_f32_16x16x32_bf16 v[64:67], a[228:231], v[52:55], v[64:67]\n"
    "v_mfma_f32_16x16x32_bf16 v[64:67], a[232:235], v[56:59], v[64:67]\n"
    "s_nop 0\n"
    "v_cvt_pk_bf16_f32 v112, v72, v73\n"
    "v_mfma_f32_16x16x32_bf16 v[64:67], a[236:239], v[60:63], v[64:67]\n"
    "v_cvt_pk_bf16_f32 v113, v74, v75\n"
    "v_mfma_f32_16x16x32_bf16 v[68:71], a[240:243], v[40:43], 0\n"
    "ds_write_b64 v8, v[112:113] offset:12288\n"
    "v_mfma_f32_16x16x32_bf16 v[68:71], a[244:247], v[44:47], v[68:71]\n"
    "v_mfma_f32_16x16x32_bf16 v[68:71], a[248:251], v[48:51], v[68:71]\n"
    "v_mfma_f32_16x16x32_bf16 v[68:71], a[252:255], v[52:55], v[68:71]\n"
    "v_mfma_f32_16x16x32_bf16 v[68:71], v[224:227], v[56:59], v[68:71]\n"
    "s_nop 0\n"
    "v_cvt_pk_bf16_f32 v116, v64, v65\n"
    "v_mfma_f32_16x16x32_bf16 v[68:71], v[228:231], v[60:63], v[68:71]\n"
    "v_cvt_pk_bf16_f32 v117, v66, v67\n"
    "v_mfma_f32_16x16x32_bf16 v[72:75], v[232:235], v[40:43], 0\n"
    "v_mfma_f32_16x16x32_bf16 v[72:75], v[236:239], v[44:47], v[72:75]\n"
    "s_waitcnt lgkmcnt(0)\n"
    "s_barrier\n"
    "v_mfma_f32_16x16x32_bf16 v[72:75], v[240:243], v[48:51], v[72:75]\n"
    "ds_read_b128 v[192:195], v2 offset:24576\n"
    "v_mfma_f32_16x16x32_bf16 v[72:75], v[244:247], v[52:55], v[72:75]\n"
    "ds_read_b128 v[196:199], v2 offset:25600\n"
    "v_cvt_pk_bf16_f32 v118, v68, v69\n"
    "v_mfma_f32_16x16x32_bf16 v[72:75], v[248:251], v[56:59], v[72:75]\n"
    "ds_read_b128 v[200:203], v2 offset:26624\n"
    "v_cvt_pk_bf16_f32 v119, v70, v71\n"
    "v_mfma_f32_16x16x32_bf16 v[72:75], v[252:255], v[60:63], v[72:75]\n"
    "ds_read_b128 v[204:207], v2 offset:27648\n"
    "buffer_store_dwordx4 v[116:119], v10, s[20:23], s47 offen nt\n"
    "ds_read_b128 v[208:211], v2 offset:28672\n"
    "ds_read_b128 v[212:215], v2 offset:29696\n"
    "ds_read_b128 v[16:19], v9 offset:0\n"
    "ds_read_b128 v[20:23], v81 offset:3072\n"
    "ds_read_b128 v[24:27], v9 offset:128\n"
    "ds_read_b128 v[28:31], v81 offset:3200\n"
    "v_cvt_pk_bf16_f32 v120, v72, v73\n"
    "s_waitcnt lgkmcnt(9)\n"
    "v_mfma_f32_16x16x32_bf16 v[64:67], a[216:219], v[192:195], 0\n"
    "v_cvt_pk_bf16_f32 v121, v74, v75\n"
    "ds_read_b128 v[32:35], v9 offset:256\n"
    "s_waitcnt lgkmcnt(9)\n"
    "v_mfma_f32_16x16x32_bf16 v[64:67], a[220:223], v[196:199], v[64:67]\n"
    "buffer_store_dwordx2 v[120:121], v11, s[20:23], s47 offen nt\n"
    "s_waitcnt lgkmcnt(8)\n"
    "v_mfma_f32_16x16x32_bf16 v[64:67], a[224:227], v[200:203], v[64:67]\n"
    "ds_read_b128 v[36:39], v81 offset:3328\n"
    "s_waitcnt lgkmcnt(8)\n"
    "v_mfma_f32_16x16x32_bf16 v[64:67], a[228:231], v[204:207], v[64:67]\n"
    "s_waitcnt lgkmcnt(7)\n"
    "v_mfma_f32_16x16x32_bf16 v[64:67], a[232:235], v[208:211], v[64:67]\n"
    "s_waitcnt lgkmcnt(6)\n"
    "v_mfma_f32_16x16x32_bf16 v[64:67], a[236:239], v[212:215], v[64:67]\n"
    "s_waitcnt lgkmcnt(5)\n"
    "buffer_store_dwordx4 v[16:19], v6, s[16:19], s44 offen offset:0 nt\n"
    "v_mfma_f32_16x16x32_bf16 v[68:71], a[240:243], v[192:195], 0\n"
    "s_waitcnt lgkmcnt(2)\n"
    "buffer_store_dwordx4 v[20:23], v7, s[16:19], s44 offen offset:0 nt\n"
    "v_mfma_f32_16x16x32_bf16 v[68:71], a[244:247], v[196:199], v[68:71]\n"
    "buffer_store_dwordx4 v[24:27], v6, s[16:19], s44 offen offset:128 nt\n"
    "v_mfma_f32_16x16x32_bf16 v[68:71], a[248:251], v[200:203], v[68:71]\n"
    "buffer_store_dwordx4 v[28:31], v7, s[16:19], s44 offen offset:128 nt\n"
    "v_mfma_f32_16x16x32_bf16 v[68:71], a[252:255], v[204:207], v[68:71]\n"
    "s_waitcnt lgkmcnt(0)\n"
    "buffer_store_dwordx4 v[32:35], v6, s[16:19], s44 offen offset:256 nt\n"
    "v_mfma_f32_16x16x32_bf16 v[68:71], v[224:227], v[208:211], v[68:71]\n"
    "buffer_store_dwordx4 v[36:39], v7, s[16:19], s44 offen offset:256 nt\n"
    "v_mfma_f32_16x16x32_bf16 v[68:71], v[228:231], v[212:215], v[68:71]\n"
    "v_cvt_pk_bf16_f32 v100, v64, v65\n"
    "v_cvt_pk_bf16_f32 v101, v66, v67\n"
    "v_mfma_f32_16x16x32_bf16 v[72:75], v[232:235], v[192:195], 0\n"
    "v_mfma_f32_16x16x32_bf16 v[72:75], v[236:239], v[196:199], v[72:75]\n"
    "v_mfma_f32_16x16x32_bf16 v[72:75], v[240:243], v[200:203], v[72:75]\n"
    "v_mfma_f32_16x16x32_bf16 v[72:75], v[244:247], v[204:207], v[72:75]\n"
    "v_mfma_f32_16x16x32_bf16 v[72:75], v[248:251], v[208:211], v[72:75]\n"
    "s_nop 0\n"
    "v_cvt_pk_bf16_f32 v102, v68, v69\n"
    "v_mfma_f32_16x16x32_bf16 v[72:75], v[252:255], v[212:215], v[72:75]\n"
    "v_cvt_pk_bf16_f32 v103, v70, v71\n"
    "buffer_store_dwordx4 v[100:103], v10, s[20:23], s48 offen nt\n"
    "s_nop 0\n"
    "s_nop 0\n"
    "s_nop 0\n"
    "s_nop 0\n"
    "s_nop 0\n"
    "s_nop 0\n"
    "v_cvt_pk_bf16_f32 v104, v72, v73\n"
    "v_cvt_pk_bf16_f32 v105, v74, v75\n"
    "buffer_store_dwordx2 v[104:105], v11, s[20:23], s48 offen nt\n"
    ".Lswin_fin%=:\n"
    : "+v"(tid)
    : "s"(__builtin_amdgcn_kernarg_segment_ptr()), "s"(blockIdx.x), "s"((unsigned)(size_t)smem)
    : "v1", "v2", "v3", "v4", "v5", "v6", "v7", "v8", "v9", "v10", "v11", "v12", "v13", "v14", "v15", "v16", "v17", "v18", "v19", "v20", "v21", "v22", "v23", "v24",
      "v25", "v26", "v27", "v28", "v29", "v30", "v31", "v32", "v33", "v34", "v35", "v36", "v37", "v38", "v39", "v40", "v41", "v42", "v43", "v44", "v45", "v46", "v47", "v48",
      "v49", "v50", "v51", "v52", "v53", "v54", "v55", "v56", "v57", "v58", "v59", "v60", "v61", "v62", "v63", "v64", "v65", "v66", "v67", "v68", "v69", "v70", "v71", "v72",
      "v73", "v74", "v75", "v76", "v77", "v78", "v79", "v80", "v81", "v82", "v83", "v84", "v85", "v86", "v87", "v88", "v89", "v90", "v91", "v92", "v93", "v94", "v95", "v96",
      "v97", "v98", "v99", "v100", "v101", "v102", "v103", "v104", "v105", "v106", "v107", "v108", "v109", "v110", "v111", "v112", "v113", "v114", "v115", "v116", "v117", "v118", "v119", "v120",
      "v121", "v122", "v123", "v124", "v125", "v126", "v127", "v128", "v129", "v130", "v131", "v132", "v133", "v134", "v135", "v136", "v137", "v138", "v139", "v140", "v141", "v142", "v143", "v144",
      "v145", "v146", "v147", "v148", "v149", "v150", "v151", "v152", "v153", "v154", "v155", "v156", "v157", "v158", "v159", "v160", "v161", "v162", "v163", "v164", "v165", "v166", "v167", "v168",
      "v169", "v170", "v171", "v172", "v173", "v174", "v175", "v176", "v177", "v178", "v179", "v180", "v181", "v182", "v183", "v184", "v185", "v186", "v187", "v188", "v189", "v190", "v191", "v192",
      "v193", "v194", "v195", "v196", "v197", "v198", "v199", "v200", "v201", "v202", "v203", "v204", "v205", "v206", "v207", "v208", "v209", "v210", "v211", "v212", "v213", "v214", "v215", "v216",
      "v217", "v218", "v219", "v220", "v221", "v222", "v223", "v224", "v225", "v226", "v227", "v228", "v229", "v230", "v231", "v232", "v233", "v234", "v235", "v236", "v237", "v238", "v239", "v240",
      "v241", "v242", "v243", "v244", "v245", "v246", "v247", "v248", "v249", "v250", "v251", "v252", "v253", "v254", "v255", "a0", "a1", "a2", "a3", "a4", "a5", "a6", "a7", "a8",
      "a9", "a10", "a11", "a12", "a13", "a14", "a15", "a16", "a17", "a18", "a19", "a20", "a21", "a22", "a23", "a24", "a25", "a26", "a27", "a28", "a29", "a30", "a31", "a32",
      "a33", "a34", "a35", "a36", "a37", "a38", "a39", "a40", "a41", "a42", "a43", "a44", "a45", "a46", "a47", "a48", "a49", "a50", "a51", "a52", "a53", "a54", "a55", "a56",
      "a57", "a58", "a59", "a60", "a61", "a62", "a63", "a64", "a65", "a66", "a67", "a68", "a69", "a70", "a71", "a72", "a73", "a74", "a75", "a76", "a77", "a78", "a79", "a80",
      "a81", "a82", "a83", "a84", "a85", "a86", "a87", "a88", "a89", "a90", "a91", "a92", "a93", "a94", "a95", "a96", "a97", "a98", "a99", "a100", "a101", "a102", "a103", "a104",
      "a105", "a106", "a107", "a108", "a109", "a110", "a111", "a112", "a113", "a114", "a115", "a116", "a117", "a118", "a119", "a120", "a121", "a122", "a123", "a124", "a125", "a126", "a127", "a128",
      "a129", "a130", "a131", "a132", "a133", "a134", "a135", "a136", "a137", "a138", "a139", "a140", "a141", "a142", "a143", "a144", "a145", "a146", "a147", "a148", "a149", "a150", "a151", "a152",
      "a153", "a154", "a155", "a156", "a157", "a158", "a159", "a160", "a161", "a162", "a163", "a164", "a165", "a166", "a167", "a168", "a169", "a170", "a171", "a172", "a173", "a174", "a175", "a176",
      "a177", "a178", "a179", "a180", "a181", "a182", "a183", "a184", "a185", "a186", "a187", "a188", "a189", "a190", "a191", "a192", "a193", "a194", "a195", "a196", "a197", "a198", "a199", "a200",
      "a201", "a202", "a203", "a204", "a205", "a206", "a207", "a208", "a209", "a210", "a211", "a212", "a213", "a214", "a215", "a216", "a217", "a218", "a219", "a220", "a221", "a222", "a223", "a224",
      "a225", "a226", "a227", "a228", "a229", "a230", "a231", "a232", "a233", "a234", "a235", "a236", "a237", "a238", "a239", "a240", "a241", "a242", "a243", "a244", "a245", "a246", "a247", "a248",
      "a249", "a250", "a251", "a252", "a253", "a254", "a255", "s0", "s1", "s2", "s3", "s4", "s5", "s6", "s7", "s8", "s9", "s10", "s11", "s12", "s13", "s14", "s15", "s16",
      "s17", "s18", "s19", "s20", "s21", "s22", "s23", "s24", "s25", "s26", "s27", "s28", "s29", "s30", "s31", "s33", "s34", "s35", "s36", "s37", "s38", "s39", "s40", "s41",
      "s42", "s43", "s44", "s45", "s46", "s47", "s48", "s49", "s50", "s51", "s52", "s53", "s54", "s55", "s56", "s57", "s58", "s59", "s60", "s61", "s62", "s63", "s64", "s65",
      "s66", "s67", "s68", "s69", "s70", "s71", "s72", "s73", "s74", "s75", "s76", "s77", "s78", "s79", "s80", "s81", "s82", "s83", "s84", "s85", "s86", "s87", "s88", "s89",
      "s90", "s91", "s92", "s93", "s94", "s95", "vcc", "scc", "memory");
}

extern "C" void kernel_launch(void* const* d_in, const int* in_sizes, int n_in, void* d_out, int out_size, void* d_ws, size_t ws_size,
                              hipStream_t stream) {
  swin_attn_kernel<<<256, 256, 0, stream>>>(d_in[0], d_in[1], d_in[2], d_in[3], d_in[4], d_in[5], d_out);
}
```
